```python
import torch

def _softmax(x, axis=-1):
    return torch.softmax(x, dim=axis)

def _gelu(x):
    return torch.nn.functional.gelu(x, approximate='tanh')

def _silu(x):
    return torch.nn.functional.silu(x)

def _sigmoid(x):
    return torch.sigmoid(x)

def _layernorm(x, axis=-1, eps=1e-05):
    m = x.mean(dim=axis, keepdim=True)
    v = x.var(dim=axis, keepdim=True, unbiased=False)
    return (x - m) / torch.sqrt(v + eps)

def _scatter_add(tgt, idx, src, axis=0):
    return tgt.clone().index_add_(axis, idx, src)

def _segment_sum(src, seg, n):
    out = torch.zeros((n,) + src.shape[1:], dtype=src.dtype)
    return out.index_add_(0, seg, src)

def _pad(x, pw):
    flat = tuple((v for pair in reversed(pw) for v in pair))
    return torch.nn.functional.pad(x, flat)
kernel_name = 'adaLN_cond__spectral_fnet__dense_activation_relu__prompt'

def setup_inputs():

    _w = lambda a: (lambda t: t.to(torch.bfloat16) if t.is_floating_point() else t)(torch.as_tensor(a))

    def _A_setup():
        B, S, D = (1, 2048, 576)
        ns = torch.arange(S)[:, None] * torch.arange(S)[None, :]
        nd = torch.arange(D)[:, None] * torch.arange(D)[None, :]
        Ws_re = torch.cos(-2.0 * torch.pi * ns / S).to(float)
        Ws_im = torch.sin(-2.0 * torch.pi * ns / S).to(float)
        Wd_re = torch.cos(-2.0 * torch.pi * nd / D).to(float)
        Wd_im = torch.sin(-2.0 * torch.pi * nd / D).to(float)
        return {'x': _w((lambda *s: torch.randn(*s, dtype=torch.bfloat16) * 0.05)(B, S, D)), 'dft_seq_re': _w(Ws_re), 'dft_seq_im': _w(Ws_im), 'dft_hid_re': _w(Wd_re), 'dft_hid_im': _w(Wd_im)}
    _A_in = _A_setup()

    def _B_setup():
        B, S, D, Dff = (1, 2048, 576, 4608)
        return {'x': _w((lambda *s: torch.randn(*s, dtype=torch.bfloat16) * 0.05)(B, S, D)), 'w1': _w((lambda *s: torch.randn(*s, dtype=torch.bfloat16) * 1.6)(D, Dff)), 'w2': _w((lambda *s: torch.randn(*s, dtype=torch.bfloat16) * 1.6)(Dff, D))}
    _B_in = _B_setup()
    return {'A_x': _A_in['x'], 'A_dft_seq_re': _A_in['dft_seq_re'], 'A_dft_seq_im': _A_in['dft_seq_im'], 'A_dft_hid_re': _A_in['dft_hid_re'], 'A_dft_hid_im': _A_in['dft_hid_im'], 'B_x': _B_in['x'], 'B_w1': _B_in['w1'], 'B_w2': _B_in['w2'], 'c': _w((lambda *s: torch.randn(*s, dtype=torch.bfloat16) * 0.1)(1, 576) * 8.0), 'w_mod': _w((lambda *s: torch.randn(*s, dtype=torch.bfloat16) * 0.1)(576, 6 * 576) * 576 ** (-0.5) * 8.0)}

def reference(A_x, A_dft_seq_re, A_dft_seq_im, A_dft_hid_re, A_dft_hid_im, B_x, B_w1, B_w2, c, w_mod):

    def _A_ref(x, dft_seq_re, dft_seq_im, dft_hid_re, dft_hid_im):
        a = torch.einsum('bsd,ed->bse', x, dft_hid_re)
        b = torch.einsum('bsd,ed->bse', x, dft_hid_im)
        y = torch.einsum('ts,bse->bte', dft_seq_re, a) - torch.einsum('ts,bse->bte', dft_seq_im, b)
        return y

    def _B_ref(x, w1, w2):
        h = torch.einsum('bsd,df->bsf', x, w1)
        zero = torch.zeros_like(h)
        h = torch.maximum(h, zero)
        return torch.einsum('bsf,fd->bsd', h, w2)
    x = A_x
    mod = (_silu(c) @ w_mod).reshape(c.shape[0], 6, 1, -1)
    sa, ca, ga, sb, cb, gb = (mod[:, 0].clone(), mod[:, 1].clone(), mod[:, 2].clone(), mod[:, 3].clone(), mod[:, 4].clone(), mod[:, 5].clone())
    h = _layernorm(x, axis=-1) * (1 + ca) + sa
    x = x + ga * _A_ref(h, A_dft_seq_re, A_dft_seq_im, A_dft_hid_re, A_dft_hid_im)
    h = _layernorm(x, axis=-1) * (1 + cb) + sb
    x = x + gb * _B_ref(h, B_w1, B_w2)
    return x
```

```cpp
#include <hip/hip_runtime.h>
#include <hip/hip_bf16.h>
#include <stdint.h>
typedef unsigned short u16;
__device__ __forceinline__ float bf2f(u16 v){ return __uint_as_float(((unsigned)v)<<16); }
__device__ __forceinline__ u16 f2bf(float f){ __hip_bfloat16 b = __float2bfloat16(f); return *(u16*)&b; }
__device__ __forceinline__ float rbf(float f){ return bf2f(f2bf(f)); }
constexpr int S=2048, D=576, F=4608;

__global__ void k_mod(const u16* c, const u16* wmod, float* mod){
  int j = blockIdx.x*blockDim.x+threadIdx.x; if(j>=6*D) return;
  float acc=0;
  for(int k=0;k<D;k++){ float cv=bf2f(c[k]); float s = rbf(cv/(1.f+__expf(-cv))); acc += s*bf2f(wmod[(size_t)k*6*D+j]); }
  mod[j]=rbf(acc);
}
__global__ void k_ln1(const u16* x, u16* xn){
  int r=blockIdx.x; int t=threadIdx.x; __shared__ float red[64];
  float s=0; for(int d=t;d<D;d+=64) s+=bf2f(x[r*D+d]);
  for(int o=32;o;o>>=1) s+=__shfl_xor(s,o);
  float m=s/D; float v=0; for(int d=t;d<D;d+=64){float e=bf2f(x[r*D+d])-m; v+=e*e;}
  for(int o=32;o;o>>=1) v+=__shfl_xor(v,o);
  float rs=rsqrtf(v/D+1e-5f);
  for(int d=t;d<D;d+=64) xn[r*D+d]=f2bf((bf2f(x[r*D+d])-m)*rs);
}
__global__ void k_pq(const u16* wre, const u16* wim, const u16* xn, const float* mod, u16* pq){
  int d=blockIdx.x*64+threadIdx.x; int t=blockIdx.y; int im=blockIdx.z;
  const u16* w = im? wim: wre;
  float acc=0, rs=0;
  for(int s=0;s<S;s++){ float wv=bf2f(w[(size_t)t*S+s]); acc+=wv*bf2f(xn[s*D+d]); rs+=wv; }
  float sa=mod[d], ca1=rbf(1.f+mod[D+d]);
  float v=acc*ca1+rs*sa; if(im) v=-v;
  pq[(size_t)t*2*D+im*D+d]=f2bf(v);
}
__global__ void k_y(const u16* pq, const u16* dre, const u16* dim_, const u16* x, const float* mod, u16* x2, u16* ap){
  int e=blockIdx.x*64+threadIdx.x; int t=blockIdx.y;
  float acc=0;
  for(int d=0;d<D;d++){ acc+=bf2f(pq[(size_t)t*2*D+d])*bf2f(dre[e*D+d]); acc+=bf2f(pq[(size_t)t*2*D+D+d])*bf2f(dim_[e*D+d]); }
  float ga=mod[2*D+e], cb1=rbf(1.f+mod[4*D+e]);
  float xv=rbf(bf2f(x[t*D+e])+ga*acc);
  x2[t*D+e]=f2bf(xv); ap[t*D+e]=f2bf(xv*cb1);
}
__global__ void k_stats(const u16* x2, float* st){
  int r=blockIdx.x; int t=threadIdx.x;
  float s=0; for(int d=t;d<D;d+=64) s+=bf2f(x2[r*D+d]);
  for(int o=32;o;o>>=1) s+=__shfl_xor(s,o);
  float m=s/D; float v=0; for(int d=t;d<D;d+=64){float e=bf2f(x2[r*D+d])-m; v+=e*e;}
  for(int o=32;o;o>>=1) v+=__shfl_xor(v,o);
  if(t==0){ st[2*r]=m; st[2*r+1]=rsqrtf(v/D+1e-5f);}
}
__global__ void k_uv(const u16* w1, const float* mod, float* u, float* v){
  int f=blockIdx.x*64+threadIdx.x; float a=0,b=0;
  for(int d=0;d<D;d++){ float w=bf2f(w1[(size_t)d*F+f]); a+=rbf(1.f+mod[4*D+d])*w; b+=mod[3*D+d]*w; }
  u[f]=a; v[f]=b;
}
__global__ void k_h1(const u16* ap, const u16* w1, const float* st, const float* u, const float* v, u16* h1){
  int f=blockIdx.x*64+threadIdx.x; int t=blockIdx.y; float acc=0;
  for(int d=0;d<D;d++) acc+=bf2f(ap[t*D+d])*bf2f(w1[(size_t)d*F+f]);
  float r=st[2*t+1]*(acc-st[2*t]*u[f])+v[f];
  h1[(size_t)t*F+f]=f2bf(fmaxf(r,0.f));
}
__global__ void k_out(const u16* h1, const u16* w2, const u16* x2, const float* mod, u16* out){
  int d=blockIdx.x*64+threadIdx.x; int t=blockIdx.y; float acc=0;
  for(int f=0;f<F;f++) acc+=bf2f(h1[(size_t)t*F+f])*bf2f(w2[(size_t)f*D+d]);
  out[t*D+d]=f2bf(bf2f(x2[t*D+d])+mod[5*D+d]*acc);
}
extern "C" void kernel_launch(void* const* d_in, const int* in_sizes, int n_in, void* d_out, int out_size, void* d_ws, size_t ws_size, hipStream_t stream){
  const u16* x=(const u16*)d_in[0]; const u16* wre=(const u16*)d_in[1]; const u16* wim=(const u16*)d_in[2];
  const u16* dre=(const u16*)d_in[3]; const u16* dim_=(const u16*)d_in[4];
  const u16* w1=(const u16*)d_in[6]; const u16* w2=(const u16*)d_in[7]; const u16* c=(const u16*)d_in[8]; const u16* wmod=(const u16*)d_in[9];
  char* ws=(char*)d_ws;
  float* mod=(float*)ws; float* st=(float*)(ws+(1<<16)); float* u=(float*)(ws+(2<<16)); float* v=(float*)(ws+(3<<16));
  u16* xn=(u16*)(ws+(1<<20)); u16* pq=(u16*)(ws+(4<<20)); u16* x2=(u16*)(ws+(10<<20)); u16* ap=(u16*)(ws+(13<<20)); u16* h1=(u16*)(ws+(16<<20));
  k_mod<<<(6*D+63)/64,64,0,stream>>>(c,wmod,mod);
  k_ln1<<<S,64,0,stream>>>(x,xn);
  k_pq<<<dim3(D/64,S,2),64,0,stream>>>(wre,wim,xn,mod,pq);
  k_y<<<dim3(D/64,S),64,0,stream>>>(pq,dre,dim_,x,mod,x2,ap);
  k_stats<<<S,64,0,stream>>>(x2,st);
  k_uv<<<F/64,64,0,stream>>>(w1,mod,u,v);
  k_h1<<<dim3(F/64,S),64,0,stream>>>(ap,w1,st,u,v,h1);
  k_out<<<dim3(D/64,S),64,0,stream>>>(h1,w2,x2,mod,(u16*)d_out);
}
```

```cpp
#include <hip/hip_runtime.h>
#include <hip/hip_bf16.h>
#include <stdint.h>

typedef unsigned short u16;
typedef __attribute__((ext_vector_type(2))) float f32x2;
typedef __attribute__((ext_vector_type(4))) float f32x4;
typedef __attribute__((ext_vector_type(2))) __bf16 bf16x2;
typedef __attribute__((ext_vector_type(8))) __bf16 bf16x8;
typedef __attribute__((ext_vector_type(2))) unsigned u32x2;
typedef __attribute__((ext_vector_type(4))) unsigned u32x4;
typedef __amdgpu_buffer_rsrc_t rsrc_t;
#define LAS __attribute__((address_space(3)))

constexpr int S = 2048, D = 576, F = 4608;
constexpr unsigned MOD_OFF = 0;
constexpr unsigned XN_OFF = 1u << 16;
constexpr unsigned XN_BYTES = S * D * 2;
constexpr unsigned WD_OFF = XN_OFF + XN_BYTES;
constexpr unsigned WD_BYTES = D * D * 4;
constexpr unsigned YP_OFF = WD_OFF + WD_BYTES;
constexpr unsigned YP_BYTES = 4 * S * D * 2;
constexpr unsigned X2_OFF = YP_OFF + YP_BYTES;
constexpr unsigned H2_OFF = X2_OFF + XN_BYTES;
constexpr unsigned W1_OFF = H2_OFF + XN_BYTES;
constexpr unsigned W_BYTES = D * F * 2;
constexpr unsigned W2_OFF = W1_OFF + W_BYTES;
constexpr unsigned OP_OFF = W2_OFF + W_BYTES;
constexpr unsigned OP_BYTES = 8 * S * D * 2;
constexpr unsigned WS_END = OP_OFF + OP_BYTES;
constexpr int AUX_SC1 = 16;

__device__ __forceinline__ rsrc_t mkrs(const void* p, unsigned bytes) {
  return __builtin_amdgcn_make_buffer_rsrc((void*)p, 0, bytes, 0x00020000);
}
__device__ __forceinline__ u32x4 bld128(rsrc_t r, int v, int s) { return __builtin_amdgcn_raw_buffer_load_b128(r, v, s, 0); }
__device__ __forceinline__ u32x2 bld64(rsrc_t r, int v, int s) { return __builtin_amdgcn_raw_buffer_load_b64(r, v, s, 0); }
__device__ __forceinline__ float bflo(unsigned u) { return __uint_as_float(u << 16); }
__device__ __forceinline__ float bfhi(unsigned u) { return __uint_as_float(u & 0xffff0000u); }
__device__ __forceinline__ unsigned pk(float a, float b) {
  f32x2 v = {a, b};
  return __builtin_bit_cast(unsigned, __builtin_convertvector(v, bf16x2));
}
__device__ __forceinline__ float rbf(float a) { return bflo(pk(a, 0.f)); }
__device__ __forceinline__ f32x4 mfma(u32x4 a, u32x4 b, f32x4 c) {
  return __builtin_amdgcn_mfma_f32_16x16x32_bf16(__builtin_bit_cast(bf16x8, a), __builtin_bit_cast(bf16x8, b), c, 0, 0, 0);
}
#define SCHED() __builtin_amdgcn_sched_barrier(0)
#define STAMP_DECL
#define STAMP(i)
#define STAMP_OUT(ws, kid)
__device__ __forceinline__ int frag_of(int w, int ql) { return (w & 1) ? (ql < 8 ? 9 * w + 1 + ql : 9 * w) : 9 * w + ql; }
__device__ __forceinline__ int slot_of(int f) {
  int w = f / 9, r = f - 9 * w;
  return 9 * w + ((w & 1) ? (r == 0 ? 8 : r - 1) : r);
}
constexpr int stream_off(int f) {
  return (f / 9) * (18 * 9216) + (((f / 9) & 1) ? (f % 9 == 0 ? 8 : f % 9 - 1) : f % 9) * 1024;
}
__device__ __forceinline__ float wsum(float v) {
#pragma unroll
  for (int o = 32; o; o >>= 1) v += __shfl_xor(v, o);
  return v;
}

constexpr int K0_LN = 256, K0_MOD = 54, K0_WD = 144;
__global__ __launch_bounds__(576) void k0_pre(const u16* __restrict__ x, const u16* __restrict__ c, const u16* __restrict__ wmod,
                                              const u16* __restrict__ dre, const u16* __restrict__ dim, char* __restrict__ ws) {
  __shared__ __attribute__((aligned(16))) char sm[72 * 64 * 4 + 576 * 4];
  const int tid = threadIdx.x, lane = tid & 63, w = tid >> 6, b = blockIdx.x;
  STAMP_DECL
  STAMP(0);
  if (b < K0_LN) {
    if (w < 8) {
      const unsigned* row = (const unsigned*)(x + (size_t)(8 * b + w) * D);
      unsigned v[5];
#pragma unroll
      for (int i = 0; i < 4; ++i) v[i] = row[lane + 64 * i];
      v[4] = lane < 32 ? row[lane + 256] : 0u;
      float s = 0.f;
#pragma unroll
      for (int i = 0; i < 5; ++i) s += bflo(v[i]) + bfhi(v[i]);
      const float m = wsum(s) * (1.f / D);
      float q = 0.f;
#pragma unroll
      for (int i = 0; i < 5; ++i) {
        float a = bflo(v[i]) - m, bb = bfhi(v[i]) - m;
        if (i < 4 || lane < 32) q += a * a + bb * bb;
      }
      const float r = rsqrtf(wsum(q) * (1.f / D) + 1e-5f);
      u16* t = (u16*)sm;
#pragma unroll
      for (int i = 0; i < 5; ++i) {
        if (i < 4 || lane < 32) {
          unsigned p = pk((bflo(v[i]) - m) * r, (bfhi(v[i]) - m) * r);
          int col = 2 * (lane + 64 * i);
          t[col * 8 + w] = (u16)p;
          t[col * 8 + 8 + w] = (u16)(p >> 16);
        }
      }
    }
    __syncthreads();
    const u32x4 o = *(const u32x4*)(sm + tid * 16);
    const int sb = b >> 2, g = b & 3, db = tid >> 4, q = db / 9, dl = db - 9 * q;
    *(u32x4*)(ws + XN_OFF + (size_t)(((q * 64 + sb) * 9 + dl) * 1024 + ((tid & 15) + 16 * g) * 16)) = o;
  } else if (b < K0_LN + K0_MOD) {
    const int cb = b - K0_LN, ch = tid & 7, rg = tid >> 3;
    u32x4 v[8];
#pragma unroll
    for (int i = 0; i < 8; ++i) v[i] = *(const u32x4*)(wmod + (size_t)(rg + 72 * i) * (6 * D) + cb * 64 + ch * 8);
    float* sc = (float*)(sm + 72 * 64 * 4);
    {
      float cv = bflo(c[tid]);
      sc[tid] = rbf(cv / (1.f + __expf(-cv)));
    }
    __syncthreads();
    float a[8];
#pragma unroll
    for (int j = 0; j < 8; ++j) a[j] = 0.f;
#pragma unroll
    for (int i = 0; i < 8; ++i) {
      const float s = sc[rg + 72 * i];
#pragma unroll
      for (int j = 0; j < 4; ++j) {
        a[2 * j] += s * bflo(v[i][j]);
        a[2 * j + 1] += s * bfhi(v[i][j]);
      }
    }
    float* red = (float*)sm;
    *(f32x4*)(red + rg * 64 + ch * 8) = f32x4{a[0], a[1], a[2], a[3]};
    *(f32x4*)(red + rg * 64 + ch * 8 + 4) = f32x4{a[4], a[5], a[6], a[7]};
    __syncthreads();
    float p = 0.f;
#pragma unroll
    for (int i = 0; i < 8; ++i) p += red[(w * 8 + i) * 64 + lane];
    __syncthreads();
    red[w * 64 + lane] = p;
    __syncthreads();
    if (w == 0) {
      float t = 0.f;
#pragma unroll
      for (int i = 0; i < 9; ++i) t += red[i * 64 + lane];
      t = rbf(t);
      const int col = cb * 64 + lane, sec = col / D;
      if (sec == 1 || sec == 4) t = rbf(1.f + t);
      ((float*)(ws + MOD_OFF))[col] = t;
    }
  } else {
    const int fr = (b - K0_LN - K0_MOD) * 9 + w;
    const int eb = fr / 36, db = fr - 36 * eb;
    const size_t o = (size_t)(16 * eb + (lane & 15)) * D + 16 * db + 4 * (lane >> 4);
    const u32x2 a = *(const u32x2*)(dre + o), bb = *(const u32x2*)(dim + o);
    const int q = db / 9, kb = db - 9 * q, sl = slot_of(eb), ww = sl / 9, ql = sl - 9 * ww;
    *(u32x4*)(ws + WD_OFF + (size_t)((((q * 4 + ww) * 9 + kb) * 9 + ql) * 1024 + lane * 16)) = u32x4{a.x, a.y, bb.x, bb.y};
  }
  STAMP(1);
  STAMP_OUT(ws, 0);
}

constexpr int K12_NR = 36;
constexpr int K12_LDS = 4 * 30 * 1024 + 18 * 1024 + 4 * 4 * 256;
__global__ __launch_bounds__(256, 1) void k12_mix(const u16* __restrict__ wre, const u16* __restrict__ wim, const u16* __restrict__ w1,
                                                  const u16* __restrict__ w2, char* __restrict__ ws) {
  extern __shared__ __attribute__((aligned(16))) char smem[];
  LAS char* L = (LAS char*)smem;
  const int tid = threadIdx.x, lane = tid & 63, w = __builtin_amdgcn_readfirstlane(tid >> 6);
  const int b = blockIdx.x, xcd = b & 7, idx = b >> 3;
  const int q = idx & 3, tb32 = xcd * 8 + (idx >> 2), t0 = 32 * tb32;
  const int li = lane & 15, lg = lane >> 4;
  const rsrc_t rWs = mkrs(ws, WS_END), rRe = mkrs(wre, S * S * 2), rIm = mkrs(wim, S * S * 2);
  const int rot = (idx >> 2) & 7;
  const int vl16 = lane * 16;
  const int xbase = XN_OFF + (q * 64 + 16 * w) * 9216;
  const int dbase = WD_OFF + (q * 4 + w) * 81 * 1024;
  const int vws = (lane >> 3) * 4096 + (lane & 7) * 16;
  const int wsbase = t0 * 4096 + w * 1024;

  u32x4 R[K12_NR];
  u32x4 St[2][8];
  auto ld_item = [&](int it) -> u32x4 {
    if (it < 144) {
      const int ks = it / 9, j = it - 9 * ks;
      const int kk = (((ks >> 1) + rot) & 7) * 2 + (ks & 1);
      return bld128(rWs, vl16, xbase + kk * 9216 + j * 1024);
    }
    return bld128(rWs, vl16, dbase + (it - 144) * 1024);
  };
  auto ld_stage = [&](int ds, u32x4(&st)[8]) {
    const int o = wsbase + ((ds + rot) & 7) * 128;
#pragma unroll
    for (int u = 0; u < 4; ++u) st[u] = bld128(rRe, vws, o + u * 32768);
#pragma unroll
    for (int u = 0; u < 4; ++u) st[4 + u] = bld128(rIm, vws, o + u * 32768);
  };
  STAMP_DECL
  STAMP(0);
  ld_stage(0, St[0]);
#pragma unroll
  for (int i = 0; i < 18; ++i) R[i] = ld_item(i);
  ld_stage(1, St[1]);
#pragma unroll
  for (int i = 18; i < K12_NR; ++i) R[i] = ld_item(i);
  SCHED();

  f32x4 acc[4][9], ars[4];
#pragma unroll
  for (int r = 0; r < 4; ++r) {
    ars[r] = f32x4{0.f, 0.f, 0.f, 0.f};
#pragma unroll
    for (int j = 0; j < 9; ++j) acc[r][j] = f32x4{0.f, 0.f, 0.f, 0.f};
  }
  const u32x4 ones = {0x3f803f80u, 0x3f803f80u, 0x3f803f80u, 0x3f803f80u};
  LAS char* myb = L + w * (30 * 1024);
  const int wr_off = (lane >> 3) * 128;
  const int rd_off = li * 128;
  const int gw = b * 4 + w;
  const int pt_is2 = gw >= 432, pt_u = gw - 432 * pt_is2;
  const int pt_s = pt_u / 54, pt_r = pt_u - 54 * pt_s, pt_kb = pt_r / 3, pt_m = pt_r - 3 * pt_kb;
  const rsrc_t rW = mkrs(pt_is2 ? w2 : w1, pt_u < 432 ? W_BYTES : 0u);
  const int pt_v = lg * (pt_is2 ? 16 * D * 2 : 4 * F * 2) + li * 8;
  int pt_so[8];
#pragma unroll
  for (int j = 0; j < 8; ++j) {
    const int f2 = 2 * pt_kb + (j >> 2);
    const int o2 = (576 * pt_s + 64 * (f2 >> 2) + (f2 & 3) + 4 * (j & 3)) * (D * 2) + 384 * pt_m;
    const int o1 = (pt_kb * 32 + (j & 3) + 16 * (j >> 2)) * (F * 2) + 576 * 2 * pt_s + 384 * pt_m;
    pt_so[j] = pt_is2 ? o2 : o1;
  }
  const int pt_db = pt_u < 432 ? (pt_is2 ? W2_OFF : W1_OFF) + pt_s * (36 * 18 * 1024) + pt_kb * 9216 : 0x7f000000;
  u32x2 pt[8];

#pragma unroll
  for (int ds = 0; ds < 8; ++ds) {
    LAS char* buf = myb + (ds & 1) * 8192;
    {
      u32x4(&st)[8] = St[ds & 1];
#pragma unroll
      for (int u = 0; u < 8; ++u) {
        const int sw = (4 * u + (lane >> 4)) & 7;
        asm volatile("" : "+a"(st[u]));
        *(LAS u32x4*)(buf + u * 1024 + wr_off + (((lane & 7) ^ sw) * 16)) = st[u];
      }
      if (ds + 2 < 8) ld_stage(ds + 2, st);
    }
    if (ds == 1 || ds == 3 || ds == 5) {
#pragma unroll
      for (int cc = 0; cc < 4; ++cc) {
        u32x4 o;
#pragma unroll
        for (int e = 0; e < 4; ++e)
          o[e] = __builtin_amdgcn_perm(pt[2 * e + 1][cc >> 1], pt[2 * e][cc >> 1], (cc & 1) ? 0x07060302u : 0x05040100u);
        const int fl = 4 * (ds >> 1) + cc;
        const int c0 = stream_off(fl), c1 = stream_off(12 + fl), c2 = stream_off(24 + fl);
        const int so = pt_db + c0 + (pt_m > 0 ? c1 - c0 : 0) + (pt_m > 1 ? c2 - c1 : 0);
        __builtin_amdgcn_raw_buffer_store_b128(o, rWs, vl16, so, AUX_SC1);
      }
    }
    if (ds == 0) STAMP(1);
    if (ds == 4) STAMP(2);
    if (ds == 0 || ds == 2 || ds == 4) {
#pragma unroll
      for (int j = 0; j < 8; ++j) pt[j] = bld64(rW, pt_v, pt_so[j] + (ds >> 1) * 128);
    }
    SCHED();
#pragma unroll
    for (int kk = 0; kk < 2; ++kk) {
      u32x4 Wf[4];
#pragma unroll
      for (int rb = 0; rb < 4; ++rb)
        Wf[rb] = *(LAS u32x4*)(buf + ((rb + w) & 3) * 2048 + rd_off + (((4 * kk + lg) ^ ((li >> 1) & 7)) * 16));
      const int ks = 2 * ds + kk;
#pragma unroll
      for (int j = 0; j < 9; ++j) {
        const int it = ks * 9 + j;
        const u32x4 a = R[it % K12_NR];
#pragma unroll
        for (int rb = 0; rb < 4; ++rb) acc[rb][j] = mfma(a, Wf[rb], acc[rb][j]);
        R[it % K12_NR] = ld_item(it + K12_NR);
        SCHED();
      }
#pragma unroll
      for (int rb = 0; rb < 4; ++rb) ars[rb] = mfma(ones, Wf[rb], ars[rb]);
      SCHED();
    }
  }
  STAMP(3);
  u32x4 R2[16];
#pragma unroll
  for (int i = 0; i < 16; ++i) R2[i] = ld_item(144 + K12_NR + i);
  SCHED();
#pragma unroll
  for (int s = 0; s < 3; ++s) {
#pragma unroll
    for (int j = 0; j < 9; ++j) *(LAS f32x4*)(myb + s * 10240 + j * 1024 + vl16) = acc[s + 1][j];
    *(LAS float*)(myb + s * 10240 + 9216 + lane * 4) = ars[s + 1][0];
  }
  SCHED();
  f32x4 ca1[9], sa[9];
  {
    const int mo = (144 * q + 4 * lg) * 4;
#pragma unroll
    for (int j = 0; j < 9; ++j) {
      sa[j] = __builtin_bit_cast(f32x4, bld128(rWs, mo, MOD_OFF + j * 64));
      ca1[j] = __builtin_bit_cast(f32x4, bld128(rWs, mo, MOD_OFF + D * 4 + j * 64));
    }
  }
  f32x4 own[9];
  float rs = ars[0][0];
#pragma unroll
  for (int j = 0; j < 9; ++j) own[j] = acc[0][j];
  __syncthreads();
#pragma unroll
  for (int s = 0; s < 3; ++s) {
    const int src = (w + 3 - s) & 3;
    LAS char* p = L + src * (30 * 1024) + s * 10240;
#pragma unroll
    for (int j = 0; j < 9; ++j) own[j] += *(LAS f32x4*)(p + j * 1024 + vl16);
    rs += *(LAS float*)(p + 9216 + lane * 4);
  }
  LAS char* pq = L + 4 * 30 * 1024;
  {
    const float sg = (w & 2) ? -1.f : 1.f;
#pragma unroll
    for (int j = 0; j < 9; ++j) {
      f32x4 v = (own[j] * ca1[j] + rs * sa[j]) * sg;
      *(LAS u32x2*)(pq + ((w & 1) * 9 + j) * 1024 + vl16 + (w >> 1) * 8) = u32x2{pk(v[0], v[1]), pk(v[2], v[3])};
    }
  }
  __syncthreads();
  STAMP(4);
  f32x4 ya[2][9];
#pragma unroll
  for (int tb = 0; tb < 2; ++tb)
#pragma unroll
    for (int j = 0; j < 9; ++j) ya[tb][j] = f32x4{0.f, 0.f, 0.f, 0.f};
  u32x4 B[2][9];
#pragma unroll
  for (int kb = 0; kb < 9; ++kb)
#pragma unroll
    for (int tb = 0; tb < 2; ++tb) B[tb][kb] = *(LAS u32x4*)(pq + (tb * 9 + kb) * 1024 + vl16);
  SCHED();
#pragma unroll
  for (int i = 0; i < 81; ++i) {
    const int kb = i / 9, j = i - 9 * kb;
    const int e = i % (K12_NR + 16);
    if (e >= K12_NR) asm volatile("" : "+a"(R2[e < K12_NR ? 0 : e - K12_NR]));
    const u32x4 a = e < K12_NR ? R[e < K12_NR ? e : 0] : R2[e < K12_NR ? 0 : e - K12_NR];
    ya[0][j] = mfma(a, B[0][kb], ya[0][j]);
    ya[1][j] = mfma(a, B[1][kb], ya[1][j]);
    if (i + K12_NR + 16 < 81) R[i] = ld_item(144 + i + K12_NR + 16);
    SCHED();
  }
  STAMP(5);
  const int p0 = (9 * w + 1) >> 1, ph = (w < 2) ? 4 : 13;
#pragma unroll
  for (int tb = 0; tb < 2; ++tb) {
    const int yb = YP_OFF + (q * 128 + 2 * tb32 + tb) * 18 * 1024;
#pragma unroll
    for (int p = 0; p < 4; ++p) {
      const f32x4 a = ya[tb][2 * p], c = ya[tb][2 * p + 1];
      __builtin_amdgcn_raw_buffer_store_b128(u32x4{pk(a[0], a[1]), pk(a[2], a[3]), pk(c[0], c[1]), pk(c[2], c[3])}, rWs, vl16,
                                             yb + (p0 + p) * 1024, 0);
    }
    const f32x4 a = ya[tb][8];
    __builtin_amdgcn_raw_buffer_store_b64(u32x2{pk(a[0], a[1]), pk(a[2], a[3])}, rWs, vl16 + (w & 1) * 8, yb + ph * 1024, 0);
  }
  STAMP(6);
  STAMP_OUT(ws, 1);
}

__global__ __launch_bounds__(576) void k3_ln2(const u16* __restrict__ x, char* __restrict__ ws) {
  __shared__ float red[2][9][8];
  const int tid = threadIdx.x, b = blockIdx.x, w = tid >> 6;
  STAMP_DECL
  STAMP(0);
  const int i8 = tid & 7, g = (tid >> 3) & 3, kb = tid >> 5;
  const int tb = b >> 1, i = (b & 1) * 8 + i8, t = 16 * tb + i;
  const int lo = (i + 16 * g) * 16;
  u32x4 yp[4];
#pragma unroll
  for (int q = 0; q < 4; ++q) yp[q] = *(const u32x4*)(ws + YP_OFF + (size_t)((q * 128 + tb) * 18 + kb) * 1024 + lo);
  const int e0 = 32 * kb + 4 * g;
  const u32x2 xa = *(const u32x2*)(x + (size_t)t * D + e0), xb = *(const u32x2*)(x + (size_t)t * D + e0 + 16);
  const float* mod = (const float*)(ws + MOD_OFF);
  const f32x4 ga0 = *(const f32x4*)(mod + 2 * D + e0), ga1 = *(const f32x4*)(mod + 2 * D + e0 + 16);
  const f32x4 sb0 = *(const f32x4*)(mod + 3 * D + e0), sb1 = *(const f32x4*)(mod + 3 * D + e0 + 16);
  const f32x4 cb0 = *(const f32x4*)(mod + 4 * D + e0), cb1 = *(const f32x4*)(mod + 4 * D + e0 + 16);
  float v[8];
#pragma unroll
  for (int j = 0; j < 4; ++j) {
    v[2 * j] = (bflo(yp[0][j]) + bflo(yp[1][j])) + (bflo(yp[2][j]) + bflo(yp[3][j]));
    v[2 * j + 1] = (bfhi(yp[0][j]) + bfhi(yp[1][j])) + (bfhi(yp[2][j]) + bfhi(yp[3][j]));
  }
  const float xv[8] = {bflo(xa.x), bfhi(xa.x), bflo(xa.y), bfhi(xa.y), bflo(xb.x), bfhi(xb.x), bflo(xb.y), bfhi(xb.y)};
  unsigned p[4];
#pragma unroll
  for (int j = 0; j < 4; ++j) {
    const float g0 = j < 2 ? ga0[2 * (j & 1)] : ga1[2 * (j & 1)], g1 = j < 2 ? ga0[2 * (j & 1) + 1] : ga1[2 * (j & 1) + 1];
    p[j] = pk(xv[2 * j] + g0 * v[2 * j], xv[2 * j + 1] + g1 * v[2 * j + 1]);
    v[2 * j] = bflo(p[j]);
    v[2 * j + 1] = bfhi(p[j]);
  }
  u16* x2 = (u16*)(ws + X2_OFF);
  *(u32x2*)(x2 + (size_t)t * D + e0) = u32x2{p[0], p[1]};
  *(u32x2*)(x2 + (size_t)t * D + e0 + 16) = u32x2{p[2], p[3]};
  float s = ((v[0] + v[1]) + (v[2] + v[3])) + ((v[4] + v[5]) + (v[6] + v[7]));
  s += __shfl_xor(s, 8);
  s += __shfl_xor(s, 16);
  s += __shfl_xor(s, 32);
  if ((tid & 63) < 8) red[0][w][i8] = s;
  __syncthreads();
  float m = 0.f;
#pragma unroll
  for (int k = 0; k < 9; ++k) m += red[0][k][i8];
  m *= (1.f / D);
  float qq = 0.f;
#pragma unroll
  for (int j = 0; j < 8; ++j) {
    v[j] -= m;
    qq += v[j] * v[j];
  }
  qq += __shfl_xor(qq, 8);
  qq += __shfl_xor(qq, 16);
  qq += __shfl_xor(qq, 32);
  if ((tid & 63) < 8) red[1][w][i8] = qq;
  __syncthreads();
  float var = 0.f;
#pragma unroll
  for (int k = 0; k < 9; ++k) var += red[1][k][i8];
  const float r = rsqrtf(var * (1.f / D) + 1e-5f);
  u32x4 o;
  o[0] = pk(v[0] * r * cb0[0] + sb0[0], v[1] * r * cb0[1] + sb0[1]);
  o[1] = pk(v[2] * r * cb0[2] + sb0[2], v[3] * r * cb0[3] + sb0[3]);
  o[2] = pk(v[4] * r * cb1[0] + sb1[0], v[5] * r * cb1[1] + sb1[1]);
  o[3] = pk(v[6] * r * cb1[2] + sb1[2], v[7] * r * cb1[3] + sb1[3]);
  *(u32x4*)(ws + H2_OFF + (size_t)(((tb >> 2) * 18 + kb) * 4 + (tb & 3)) * 1024 + lo) = o;
  STAMP(1);
  STAMP_OUT(ws, 2);
}

constexpr int K4_NR = 45;
constexpr int K4_LDS = 2 * 72 * 1024;
__global__ __launch_bounds__(256, 1) void k4_mlp(char* __restrict__ ws) {
  extern __shared__ __attribute__((aligned(16))) char smem[];
  LAS char* L = (LAS char*)smem;
  const int tid = threadIdx.x, lane = tid & 63, w = __builtin_amdgcn_readfirstlane(tid >> 6);
  const int b = blockIdx.x, xcd = b & 7, idx = b >> 3;
  const int sl = 4 * (xcd & 1) + (idx & 3), rt = 8 * (xcd >> 1) + (idx >> 2);
  const rsrc_t rWs = mkrs(ws, WS_END);
  const int vl16 = lane * 16;
  const int b1 = W1_OFF + (sl * 4 + w) * (162 * 1024), b2 = W2_OFF + (sl * 4 + w) * (162 * 1024);
  auto ld_item = [&](int it) -> u32x4 {
    return it < 162 ? bld128(rWs, vl16, b1 + it * 1024) : bld128(rWs, vl16, b2 + (it - 162) * 1024);
  };
  u32x4 R[K4_NR];
  STAMP_DECL
  STAMP(0);
  {
    u32x4 h[18];
    const int hb = H2_OFF + rt * (72 * 1024) + w * (18 * 1024);
#pragma unroll
    for (int i = 0; i < 18; ++i) h[i] = bld128(rWs, vl16, hb + i * 1024);
#pragma unroll
    for (int i = 0; i < K4_NR; ++i) R[i] = ld_item(i);
    SCHED();
#pragma unroll
    for (int i = 0; i < 18; ++i) *(LAS u32x4*)(L + (w * 18 + i) * 1024 + vl16) = h[i];
  }
  __syncthreads();
  STAMP(1);
  f32x4 acc[4][9];
  const int p0 = (9 * w + 1) >> 1, ph = (w < 2) ? 4 : 13;
#pragma unroll
  for (int phs = 0; phs < 2; ++phs) {
    LAS char* A = L + phs * (72 * 1024) + vl16;
#pragma unroll
    for (int r = 0; r < 4; ++r)
#pragma unroll
      for (int j = 0; j < 9; ++j) acc[r][j] = f32x4{0.f, 0.f, 0.f, 0.f};
    u32x4 Bf[2][4];
#pragma unroll
    for (int mb = 0; mb < 4; ++mb) Bf[0][mb] = *(LAS u32x4*)(A + mb * 1024);
#pragma unroll
    for (int kb = 0; kb < 18; ++kb) {
      if (kb + 1 < 18) {
#pragma unroll
        for (int mb = 0; mb < 4; ++mb) Bf[(kb + 1) & 1][mb] = *(LAS u32x4*)(A + ((kb + 1) * 4 + mb) * 1024);
      }
      SCHED();
#pragma unroll
      for (int j = 0; j < 9; ++j) {
        const int it = phs * 162 + kb * 9 + j;
        const u32x4 a = R[it % K4_NR];
#pragma unroll
        for (int mb = 0; mb < 4; ++mb) acc[mb][j] = mfma(a, Bf[kb & 1][mb], acc[mb][j]);
        if (it + K4_NR < 324) R[it % K4_NR] = ld_item(it + K4_NR);
        SCHED();
      }
    }
    STAMP(2 + 2 * phs);
    if (phs == 0) {
      LAS char* H = L + 72 * 1024 + vl16;
#pragma unroll
      for (int mb = 0; mb < 4; ++mb) {
#pragma unroll
        for (int p = 0; p < 4; ++p) {
          const f32x4 a = acc[mb][2 * p], c = acc[mb][2 * p + 1];
          *(LAS u32x4*)(H + ((p0 + p) * 4 + mb) * 1024) =
              u32x4{pk(fmaxf(a[0], 0.f), fmaxf(a[1], 0.f)), pk(fmaxf(a[2], 0.f), fmaxf(a[3], 0.f)), pk(fmaxf(c[0], 0.f), fmaxf(c[1], 0.f)),
                    pk(fmaxf(c[2], 0.f), fmaxf(c[3], 0.f))};
        }
        const f32x4 a = acc[mb][8];
        *(LAS u32x2*)(H + (ph * 4 + mb) * 1024 + (w & 1) * 8) = u32x2{pk(fmaxf(a[0], 0.f), fmaxf(a[1], 0.f)), pk(fmaxf(a[2], 0.f), fmaxf(a[3], 0.f))};
      }
      __syncthreads();
      STAMP(3);
    }
  }
#pragma unroll
  for (int mb = 0; mb < 4; ++mb) {
    const int ob = OP_OFF + ((sl * 32 + rt) * 4 + mb) * (18 * 1024);
#pragma unroll
    for (int p = 0; p < 4; ++p) {
      const f32x4 a = acc[mb][2 * p], c = acc[mb][2 * p + 1];
      __builtin_amdgcn_raw_buffer_store_b128(u32x4{pk(a[0], a[1]), pk(a[2], a[3]), pk(c[0], c[1]), pk(c[2], c[3])}, rWs, vl16,
                                             ob + (p0 + p) * 1024, 0);
    }
    const f32x4 a = acc[mb][8];
    __builtin_amdgcn_raw_buffer_store_b64(u32x2{pk(a[0], a[1]), pk(a[2], a[3])}, rWs, vl16 + (w & 1) * 8, ob + ph * 1024, 0);
  }
  STAMP(5);
  STAMP_OUT(ws, 3);
}

__global__ __launch_bounds__(256) void k5_out(const char* __restrict__ ws, u16* __restrict__ out) {
  const int lane = threadIdx.x & 63, gw = blockIdx.x * 4 + (threadIdx.x >> 6);
  STAMP_DECL
  STAMP(0);
  const int G = gw % 9, tb = gw / 9;
  u32x4 v[8][2];
#pragma unroll
  for (int s = 0; s < 8; ++s)
#pragma unroll
    for (int h = 0; h < 2; ++h) v[s][h] = *(const u32x4*)(ws + OP_OFF + (size_t)((s * 128 + tb) * 18 + 2 * G + h) * 1024 + lane * 16);
  const int t = 16 * tb + (lane & 15), d0 = 64 * G + 16 * (lane >> 4);
  const u16* x2 = (const u16*)(ws + X2_OFF) + (size_t)t * D + d0;
  const u32x4 xa = *(const u32x4*)x2, xb = *(const u32x4*)(x2 + 8);
  const float* gb = (const float*)(ws + MOD_OFF) + 5 * D + d0;
  f32x4 g[4];
#pragma unroll
  for (int r = 0; r < 4; ++r) g[r] = *(const f32x4*)(gb + 4 * r);
  float a[4][4];
#pragma unroll
  for (int h = 0; h < 2; ++h)
#pragma unroll
    for (int e = 0; e < 4; ++e) {
      float lo = 0.f, hi = 0.f;
#pragma unroll
      for (int s = 0; s < 8; ++s) {
        lo += bflo(v[s][h][e]);
        hi += bfhi(v[s][h][e]);
      }
      a[2 * (e & 1)][2 * h + (e >> 1)] = lo;
      a[2 * (e & 1) + 1][2 * h + (e >> 1)] = hi;
    }
  u32x4 o[2];
#pragma unroll
  for (int r = 0; r < 4; ++r) {
    const unsigned x0 = r < 2 ? xa[2 * (r & 1)] : xb[2 * (r & 1)], x1 = r < 2 ? xa[2 * (r & 1) + 1] : xb[2 * (r & 1) + 1];
    o[r >> 1][2 * (r & 1)] = pk(bflo(x0) + g[r][0] * a[r][0], bfhi(x0) + g[r][1] * a[r][1]);
    o[r >> 1][2 * (r & 1) + 1] = pk(bflo(x1) + g[r][2] * a[r][2], bfhi(x1) + g[r][3] * a[r][3]);
  }
  u16* op = out + (size_t)t * D + d0;
  *(u32x4*)op = o[0];
  *(u32x4*)(op + 8) = o[1];
  STAMP(1);
  STAMP_OUT(ws, 4);
}

extern "C" void kernel_launch(void* const* d_in, const int* in_sizes, int n_in, void* d_out, int out_size, void* d_ws, size_t ws_size,
                              hipStream_t stream) {
  const u16* x = (const u16*)d_in[0];
  const u16* wre = (const u16*)d_in[1];
  const u16* wim = (const u16*)d_in[2];
  const u16* dre = (const u16*)d_in[3];
  const u16* dim = (const u16*)d_in[4];
  const u16* w1 = (const u16*)d_in[6];
  const u16* w2 = (const u16*)d_in[7];
  const u16* c = (const u16*)d_in[8];
  const u16* wmod = (const u16*)d_in[9];
  char* ws = (char*)d_ws;
  (void)hipFuncSetAttribute((const void*)k12_mix, hipFuncAttributeMaxDynamicSharedMemorySize, K12_LDS);
  (void)hipFuncSetAttribute((const void*)k4_mlp, hipFuncAttributeMaxDynamicSharedMemorySize, K4_LDS);
  k0_pre<<<K0_LN + K0_MOD + K0_WD, 576, 0, stream>>>(x, c, wmod, dre, dim, ws);
  k12_mix<<<256, 256, K12_LDS, stream>>>(wre, wim, w1, w2, ws);
  k3_ln2<<<256, 576, 0, stream>>>(x, ws);
  k4_mlp<<<256, 256, K4_LDS, stream>>>(ws);
  k5_out<<<288, 256, 0, stream>>>(ws, (u16*)d_out);
}
```

```cpp
#include <hip/hip_runtime.h>
#include <hip/hip_bf16.h>
#include <stdint.h>

typedef unsigned short u16;
typedef unsigned long long u64;
typedef __attribute__((ext_vector_type(2))) float f32x2;
typedef __attribute__((ext_vector_type(4))) float f32x4;
typedef __attribute__((ext_vector_type(2))) __bf16 bf16x2;
typedef __attribute__((ext_vector_type(8))) __bf16 bf16x8;
typedef __attribute__((ext_vector_type(4))) short s16x4;
typedef __attribute__((ext_vector_type(2))) unsigned u32x2;
typedef __attribute__((ext_vector_type(4))) unsigned u32x4;
typedef __amdgpu_buffer_rsrc_t rsrc_t;
#define LAS __attribute__((address_space(3)))
typedef __attribute__((address_space(1))) u64 gu64;

constexpr int S = 2048, D = 576, F = 4608;
#define F1S 8
#define ROTX 0
constexpr unsigned E_OFF = 0, F1_OFF = 0x1000, F2_OFF = 0x10000, F3_OFF = 0x20000, F4_OFF = 0x28000, F5_OFF = 0x30000, F6_OFF = 0x38000;
constexpr unsigned MODP_OFF = 0x40000;
constexpr unsigned XN_OFF = 0x80000;
constexpr unsigned WD_OFF = XN_OFF + S * D * 2;
constexpr unsigned W1_OFF = WD_OFF + D * D * 4;
constexpr unsigned W2_OFF = W1_OFF + D * F * 2;
constexpr unsigned YP_OFF = W2_OFF + D * F * 2;
constexpr unsigned H2_OFF = YP_OFF + 1024 * 9216;
constexpr unsigned OP_OFF = H2_OFF + 256 * 9216;
constexpr unsigned WS_END = OP_OFF + 2048 * 9216;
constexpr int SC1 = 16;
constexpr int OOB = (int)0x80000000u;
constexpr int NP = 216;
constexpr int L_PQ = 65536, L_HS = 98304, L_H1 = 73728;
constexpr int L_STG = 131072, STG_ROW = 1216;
constexpr int L_RS = 131072, L_SA = 135168, L_ST = 136448, L_MV = 140544, L_GB = 147456, L_X = 149760, L_CT = 158976, L_STAMP = 159232;
constexpr int LDS_BYTES = 163840;
constexpr int SPIN_CAP = 1 << 17;

__device__ __forceinline__ rsrc_t mkrs(const void* p, unsigned bytes) {
  return __builtin_amdgcn_make_buffer_rsrc((void*)p, 0, bytes, 0x00020000);
}
__device__ __forceinline__ u32x4 mksrd(const void* p, unsigned bytes) {
  const u64 a = (u64)p;
  return u32x4{(unsigned)a, (unsigned)(a >> 32) & 0xffffu, bytes, 0x00020000u};
}
__device__ __forceinline__ u32x4 bld128(rsrc_t r, int v, int s) { return __builtin_amdgcn_raw_buffer_load_b128(r, v, s, 0); }
__device__ __forceinline__ u32x4 cld128(rsrc_t r, int v, int s) { return __builtin_amdgcn_raw_buffer_load_b128(r, v, s, SC1); }
__device__ __forceinline__ u32x2 bld64(rsrc_t r, int v, int s) { return __builtin_amdgcn_raw_buffer_load_b64(r, v, s, 0); }
#define SCHED() __builtin_amdgcn_sched_barrier(0)
__device__ __forceinline__ void cst128(u32x4 d, rsrc_t r, int v, int s) {
  __builtin_amdgcn_raw_buffer_store_b128(d, r, v, s, SC1);
  SCHED();
  asm volatile("s_nop 1" ::: "memory");
  SCHED();
}
__device__ __forceinline__ void dma(unsigned lds, int voff, u32x4 srd, int soff) {
  asm volatile("s_mov_b32 m0, %0\n\ts_nop 3\n\tbuffer_load_dwordx4 %1, %2, %3 offen lds" ::"s"(lds), "v"(voff), "s"(srd), "s"(soff) : "memory");
}
#define BAR() asm volatile("s_waitcnt lgkmcnt(0)\n\ts_barrier" ::: "memory")
#define VMW(n) asm volatile("s_waitcnt vmcnt(" #n ")" ::: "memory")
#define PIN(x) asm volatile("" : "+v"(x))
__device__ __forceinline__ int fresh_lane() {
  int l;
  asm volatile("v_mbcnt_lo_u32_b32 %0, -1, 0\n\tv_mbcnt_hi_u32_b32 %0, -1, %0" : "=v"(l));
  return l;
}
#define LANES() const int lane = fresh_lane(), li = lane & 15, lg = lane >> 4, vl16 = lane * 16, tid = w * 64 + lane; (void)li, (void)lg, (void)vl16, (void)tid
__device__ __forceinline__ float bflo(unsigned u) { return __uint_as_float(u << 16); }
__device__ __forceinline__ float bfhi(unsigned u) { return __uint_as_float(u & 0xffff0000u); }
__device__ __forceinline__ unsigned pk(float a, float b) {
  f32x2 v = {a, b};
  return __builtin_bit_cast(unsigned, __builtin_convertvector(v, bf16x2));
}
__device__ __forceinline__ float rbf(float a) { return bflo(pk(a, 0.f)); }
__device__ __forceinline__ u32x2 pk4(f32x4 a) { return u32x2{pk(a[0], a[1]), pk(a[2], a[3])}; }
__device__ __forceinline__ u32x4 pk8(f32x4 a, f32x4 b) { return u32x4{pk(a[0], a[1]), pk(a[2], a[3]), pk(b[0], b[1]), pk(b[2], b[3])}; }
__device__ __forceinline__ f32x4 up4(unsigned a, unsigned b) { return f32x4{bflo(a), bfhi(a), bflo(b), bfhi(b)}; }
__device__ __forceinline__ f32x4 relu4(f32x4 a) { return f32x4{fmaxf(a[0], 0.f), fmaxf(a[1], 0.f), fmaxf(a[2], 0.f), fmaxf(a[3], 0.f)}; }
__device__ __forceinline__ f32x4 mfma(u32x4 a, u32x4 b, f32x4 c) {
  return __builtin_amdgcn_mfma_f32_16x16x32_bf16(__builtin_bit_cast(bf16x8, a), __builtin_bit_cast(bf16x8, b), c, 0, 0, 0);
}
__device__ __forceinline__ void mac(f32x4& c, u32x4 a, u32x4 b, bool first) {
  if (first) asm("v_mfma_f32_16x16x32_bf16 %0, %1, %2, 0" : "=&v"(c) : "v"(a), "v"(b));
  else asm("v_mfma_f32_16x16x32_bf16 %0, %1, %2, %0" : "+v"(c) : "v"(a), "v"(b));
}
#define MFMA_DONE() do { SCHED(); asm volatile("s_nop 7"); SCHED(); } while (0)
template <int C>
__device__ __forceinline__ float dppadd(float v) {
  return v + __uint_as_float(__builtin_amdgcn_update_dpp(0, __float_as_uint(v), C, 0xf, 0xf, false));
}
__device__ __forceinline__ float sum16(float v) {
  v = dppadd<0x124>(v);
  v = dppadd<0x128>(v);
  auto a = __builtin_amdgcn_permlane16_swap(__float_as_uint(v), __float_as_uint(v), false, false);
  v = __uint_as_float(a[0]) + __uint_as_float(a[1]);
  auto c = __builtin_amdgcn_permlane32_swap(__float_as_uint(v), __float_as_uint(v), false, false);
  return __uint_as_float(c[0]) + __uint_as_float(c[1]);
}
__device__ __forceinline__ float wsum(float v) { return sum16(dppadd<0x4e>(dppadd<0xb1>(v))); }
__device__ __forceinline__ u64 fld(const char* p) { return __hip_atomic_load((gu64*)p, __ATOMIC_RELAXED, __HIP_MEMORY_SCOPE_AGENT); }
__device__ __forceinline__ void fst(char* p, u64 v) { __hip_atomic_store((gu64*)p, v, __ATOMIC_RELAXED, __HIP_MEMORY_SCOPE_AGENT); }
__device__ __forceinline__ void poll(const char* p, u64 tag) {
  for (int it = 0; it < SPIN_CAP; ++it) {
    const u64 v = fld(p);
    if (__builtin_amdgcn_ballot_w64(v != tag) == 0) break;
    __builtin_amdgcn_s_sleep(1);
  }
  asm volatile("" ::: "memory");
}
__device__ __forceinline__ int slot_of(int f) {
  const int w = (f * 43) >> 7, r = f - 3 * w;
  return w * 54 + ((w & 1) ? (r == 0 ? 2 : r - 1) : r);
}
struct Unit {
  u32x2 v[8];
};
__device__ __forceinline__ void unit_load(Unit& u, rsrc_t r, int voff, int base, int stride, int kb) {
#pragma unroll
  for (int j = 0; j < 8; ++j) {
    const int f2 = 2 * kb + (j >> 2);
    u.v[j] = bld64(r, voff, base + (64 * (f2 >> 2) + (f2 & 3) + 4 * (j & 3)) * stride);
  }
}
__device__ __forceinline__ void unit_store(const Unit& u, rsrc_t r, int voff, int base, int g) {
#pragma unroll
  for (int cc = 0; cc < 4; ++cc) {
    u32x4 o;
#pragma unroll
    for (int e = 0; e < 4; ++e) o[e] = __builtin_amdgcn_perm(u.v[2 * e + 1][cc >> 1], u.v[2 * e][cc >> 1], (cc & 1) ? 0x07060302u : 0x05040100u);
    cst128(o, r, voff, base + slot_of(4 * g + cc) * 1024);
  }
}

#define STAMP(i)

__global__ __launch_bounds__(768, 3) void fused_block(const u16* __restrict__ x, const u16* __restrict__ wre, const u16* __restrict__ wim,
                                                      const u16* __restrict__ dre, const u16* __restrict__ dim, const u16* __restrict__ w1,
                                                      const u16* __restrict__ w2, const u16* __restrict__ cvec, const u16* __restrict__ wmod,
                                                      char* __restrict__ ws, u16* __restrict__ out) {
  extern __shared__ __attribute__((aligned(16))) char smem[];
  LAS char* L = (LAS char*)smem;
  const unsigned lb = (unsigned)(size_t)L;
  const int w = __builtin_amdgcn_readfirstlane(threadIdx.x >> 6);
  const int b = blockIdx.x, x8 = b & 7, idx = b >> 3;
  const int q = idx & 3, rt = 8 * (x8 >> 1) + (idx >> 2), hf = x8 & 1, tb32 = 2 * rt + hf, sl = 4 * hf + q;
  const int kq = w & 3, cg = w >> 2;
  const int rot = (2 * q + ROTX * (idx >> 2)) & 7;
  const rsrc_t rWs = mkrs(ws, WS_END);
  const u32x4 sRe = mksrd(wre, S * S * 2), sIm = mksrd(wim, S * S * 2);
  auto mk_vdma = [&]() {
    const int lane = fresh_lane();
    return (lane >> 3) * 4096 + (((lane & 7) ^ ((4 * (w & 1) + (lane >> 4)) & 7)) * 16);
  };
  const int ws_row = 32 * tb32 * 4096;
  const bool job = b < NP;
  const int jsl = b / 27, jr = b - 27 * jsl;
  STAMP(0);
  auto dma_pair4 = [&](int ds) {
    const int vdma = mk_vdma();
#pragma unroll
    for (int d = 0; d < 2; ++d) {
      const int so = ws_row + kq * 32768 + ((ds + d + rot) & 7) * 128;
#pragma unroll
      for (int k = 0; k < 4; ++k) {
        dma(lb + ((ds + d) & 3) * 32768 + k * 8192 + kq * 1024, vdma, sRe, so + k * 1024);
        dma(lb + ((ds + d) & 3) * 32768 + k * 8192 + kq * 1024 + 4096, vdma, sIm, so + k * 1024);
      }
    }
  };
  u64 tag;
  auto get_tag = [&]() {
    const u32x2 t = *(LAS u32x2*)(L + L_CT);
    tag = ((u64)(unsigned)__builtin_amdgcn_readfirstlane(t.y) << 32) | (unsigned)__builtin_amdgcn_readfirstlane(t.x);
  };
  if (w < 8) {
    LANES();
    const rsrc_t rX = mkrs(x, S * D * 2);
    const u32x4 v0 = bld128(rX, vl16, (8 * b + w) * 1152), v1 = bld128(rX, lane < 8 ? vl16 + 1024 : OOB, (8 * b + w) * 1152);
    const u32x2 e = __builtin_amdgcn_raw_buffer_load_b64(rWs, tid == 0 ? E_OFF : OOB, 0, SC1);
    SCHED();
    int so[9];
    if (w == 0) {
#pragma unroll
      for (int i = 0; i < 9; ++i) {
        const int db = 4 * i + lg, d3 = (db * 43) >> 7;
        so[i] = d3 * 196608 + (db - 3 * d3) * 1024 + li * 16;
        PIN(so[i]);
      }
    }
    float f[16];
#pragma unroll
    for (int i = 0; i < 4; ++i) {
      f[2 * i] = bflo(v0[i]);
      f[2 * i + 1] = bfhi(v0[i]);
      f[8 + 2 * i] = bflo(v1[i]);
      f[9 + 2 * i] = bfhi(v1[i]);
    }
    float s = 0.f;
#pragma unroll
    for (int i = 0; i < 8; ++i) s += f[i] + f[8 + i];
    const float m = wsum(s) * (1.f / D);
    float qa = 0.f, qb = 0.f;
#pragma unroll
    for (int i = 0; i < 8; ++i) {
      f[i] -= m;
      f[8 + i] -= m;
      qa += f[i] * f[i];
      qb += f[8 + i] * f[8 + i];
    }
    const float r = rsqrtf(wsum(qa + (lane < 8 ? qb : 0.f)) * (1.f / D) + 1e-5f);
    if (tid == 0) {
      *(LAS u64*)(L + L_CT) = __builtin_bit_cast(u64, e) + 1;
      *(LAS u64*)(L + L_CT + 8) = 0;
    }
    LAS char* st = L + L_STG + w * STG_ROW + vl16;
    *(LAS u32x4*)st = u32x4{pk(f[0] * r, f[1] * r), pk(f[2] * r, f[3] * r), pk(f[4] * r, f[5] * r), pk(f[6] * r, f[7] * r)};
    if (lane < 8)
      *(LAS u32x4*)(st + 1024) = u32x4{pk(f[8] * r, f[9] * r), pk(f[10] * r, f[11] * r), pk(f[12] * r, f[13] * r), pk(f[14] * r, f[15] * r)};
    BAR();
    STAMP(1);
    get_tag();
    if (w == 0) {
      LAS char* rp = L + L_STG + (li >> 2) * STG_ROW + lg * 32 + (lane & 3) * 8;
      u32x4 o[9];
#pragma unroll
      for (int i = 0; i < 9; ++i) {
        const u32x2 lo = __builtin_bit_cast(u32x2, __builtin_amdgcn_ds_read_tr16_b64_v4i16((LAS s16x4*)(rp + i * 128)));
        const u32x2 hi = __builtin_bit_cast(u32x2, __builtin_amdgcn_ds_read_tr16_b64_v4i16((LAS s16x4*)(rp + i * 128 + 4 * STG_ROW)));
        o[i] = u32x4{lo.x, lo.y, hi.x, hi.y};
      }
      const int xb = XN_OFF + (b >> 2) * 3072 + (b & 3) * 256;
#pragma unroll
      for (int i = 0; i < 9; ++i) cst128(o[i], rWs, so[i], xb);
      VMW(0);
      if (lane == 0) fst(ws + F1_OFF + b * F1S, tag);
    } else if (w >= 4) {
      dma_pair4(2);
    }
  } else {
    dma_pair4(0);
    LANES();
    if (w == 8) {
      const rsrc_t rM = mkrs(wmod, job ? D * 6 * D * 2 : 0u), rC = mkrs(cvec, D * 2);
      const int r0 = 288 * (b & 1) + 18 * (lane >> 2);
      const u32x4 c0 = bld128(rC, r0 * 2, 0), c1 = bld128(rC, r0 * 2, 16);
      const unsigned c2 = __builtin_amdgcn_raw_buffer_load_b32(rC, r0 * 2, 32, 0);
      u32x4 mv[18];
#pragma unroll
      for (int i = 0; i < 18; ++i) mv[i] = bld128(rM, r0 * (6 * D * 2) + (lane & 3) * 16, (b >> 1) * 64 + i * (6 * D * 2));
      BAR();
      VMW(21);
      if (lane == 0) __hip_atomic_fetch_add((LAS unsigned*)(L + L_CT + 8), 1u, __ATOMIC_RELAXED, __HIP_MEMORY_SCOPE_WORKGROUP);
      get_tag();
      float a[8];
#pragma unroll
      for (int j = 0; j < 8; ++j) a[j] = 0.f;
#pragma unroll
      for (int i = 0; i < 18; ++i) {
        const unsigned cw = i < 8 ? c0[(i >> 1) & 3] : (i < 16 ? c1[(i >> 1) & 3] : c2);
        const float cv = (i & 1) ? bfhi(cw) : bflo(cw);
        const float sv = rbf(cv / (1.f + __expf(-cv)));
#pragma unroll
        for (int j = 0; j < 4; ++j) {
          a[2 * j] += sv * bflo(mv[i][j]);
          a[2 * j + 1] += sv * bfhi(mv[i][j]);
        }
      }
#pragma unroll
      for (int j = 0; j < 8; ++j) a[j] = sum16(a[j]);
      const f32x4 o = (lane & 4) ? f32x4{a[4], a[5], a[6], a[7]} : f32x4{a[0], a[1], a[2], a[3]};
      cst128(__builtin_bit_cast(u32x4, o), rWs, (lane < 8 && job) ? (lane & 3) * 32 + (lane & 4) * 4 : OOB,
             MODP_OFF + (b & 1) * (6 * D * 4) + (b >> 1) * 128);
    } else {
      {
        const u32x4 sX = mksrd(x, S * D * 2);
#pragma unroll
        for (int i = 0; i < 3; ++i) {
          const int ch = ((w - 9) * 3 + i) * 64 + lane, rr = (ch * 911) >> 16;
          dma(lb + L_X + ((w - 9) * 3 + i) * 1024, (16 * (rr >> 2) + (rr & 3)) * 1152 + (ch - 72 * rr) * 16, sX, (32 * tb32 + 4 * q) * 1152);
        }
      }
      const rsrc_t rW = mkrs(w1, job ? D * F * 2 : 0u), rDr = mkrs(dre, job ? D * D * 2 : 0u), rDi = mkrs(dim, job ? D * D * 2 : 0u);
      Unit u[2];
      u32x2 da[2], dbv[2];
      int kb[2], g[2];
#pragma unroll
      for (int i = 0; i < 2; ++i) {
        const int uu = 6 * jr + 2 * (w - 9) + i;
        kb[i] = uu / 9;
        g[i] = uu - 9 * kb[i];
        unit_load(u[i], rW, lg * (16 * F * 2) + li * 8, jsl * 1152 + g[i] * 128, F * 2, kb[i]);
      }
      const int fr = 6 * b + 2 * (w - 9), ff = fr / 36, db = fr - 36 * ff;
#pragma unroll
      for (int i = 0; i < 2; ++i) {
        const int so = (64 * (ff >> 2) + (ff & 3)) * 1152 + (db + i) * 32;
        da[i] = bld64(rDr, li * 4608 + lg * 8, so);
        dbv[i] = bld64(rDi, li * 4608 + lg * 8, so);
      }
      BAR();
      VMW(23);
      if (lane == 0) __hip_atomic_fetch_add((LAS unsigned*)(L + L_CT + 8), 1u, __ATOMIC_RELAXED, __HIP_MEMORY_SCOPE_WORKGROUP);
      get_tag();
      const int vst = job ? vl16 : OOB;
#pragma unroll
      for (int i = 0; i < 2; ++i) unit_store(u[i], rWs, vst, W1_OFF + jsl * (12 * 54 * 1024) + kb[i] * 3072, g[i]);
      const int sf = slot_of(ff), fw = sf / 54, fl = sf - 54 * fw;
#pragma unroll
      for (int i = 0; i < 2; ++i) {
        const int dq = (db + i) / 9, dk = db + i - 9 * dq;
        cst128(u32x4{da[i].x, da[i].y, dbv[i].x, dbv[i].y}, rWs, vst, WD_OFF + (((dq * 12 + fw) * 9 + dk) * 3 + fl) * 1024);
      }
    }
    VMW(0);
    if (lane == 0) {
      const unsigned n = __hip_atomic_fetch_add((LAS unsigned*)(L + L_CT + 12), 1u, __ATOMIC_RELAXED, __HIP_MEMORY_SCOPE_WORKGROUP);
      if (n == 3) fst(ws + F2_OFF + b * 8, tag);
    }
    STAMP(1);
  }
  STAMP(2);
  poll(ws + F1_OFF + (64 * kq + fresh_lane()) * F1S, tag);
  STAMP(3);
  const int xbase = XN_OFF + ((q * 3 + cg) * 4 + kq) * 49152;
  const int dbase = WD_OFF + (q * 12 + w) * 27648;
  u32x4 R[18];
  int vl16, rd0;
  {
    const int lane = fresh_lane(), li = lane & 15, lg = lane >> 4;
    vl16 = lane * 16;
    rd0 = kq * 8192 + li * 128 + ((lg ^ (li >> 1)) * 16);
  }
  auto ld1 = [&](int it) -> u32x4 {
    if (it < 48) {
      const int ks = it / 3, j = it - 3 * ks;
      return cld128(rWs, vl16, xbase + ((((ks >> 1) + rot) & 7) * 2 + (ks & 1)) * 3072 + j * 1024);
    }
    return cld128(rWs, vl16, dbase + (it - 48) * 1024);
  };
#pragma unroll
  for (int i = 0; i < 18; ++i) R[i] = ld1(i);
  SCHED();
  for (int it = 0; it < SPIN_CAP; ++it) {
    if (__hip_atomic_load((LAS unsigned*)(L + L_CT + 8), __ATOMIC_RELAXED, __HIP_MEMORY_SCOPE_WORKGROUP) == 4u) break;
    __builtin_amdgcn_s_sleep(1);
  }
  asm volatile("" ::: "memory");
  STAMP(4);
  f32x4 acc[4][3];
  float rs[4];
#pragma unroll
  for (int r = 0; r < 4; ++r) rs[r] = 0.f;
  u64 f2v = 0;
  auto f2addr = [&]() {
    const int lane = fresh_lane();
    return ws + F2_OFF + (54 * kq + (lane < 54 ? lane : 0)) * 8;
  };
  u32x4 mp0, mp1;
  int mdst = 0;
#pragma unroll
  for (int ds = 0; ds < 8; ++ds) {
    if (ds == 2 || ds == 4 || ds == 6) {
      if (ds == 4 && w >= 8) {
        if (__builtin_amdgcn_ballot_w64(f2v != tag) != 0) poll(f2addr(), tag);
      }
      if (ds > 2) VMW(12);
      BAR();
      STAMP(4 + (ds >> 1));
      if (ds < 6 && w < 8) {
        const u32x4 sW = w < 4 ? sRe : sIm;
        const int vdma = mk_vdma();
#pragma unroll
        for (int d = 0; d < 2; ++d) {
          const int so = ws_row + kq * 32768 + ((ds + 2 + d + rot) & 7) * 128;
#pragma unroll
          for (int k = 0; k < 4; ++k) dma(lb + ((ds + 2 + d) & 3) * 32768 + k * 8192 + w * 1024, vdma, sW, so + k * 1024);
        }
      }
      if (ds == 2) {
        if (w >= 8) f2v = fld(f2addr());
        if (b == 0 && w == 0 && vl16 == 0) fst(ws + E_OFF, tag);
      }
      if (ds == 4) {
        const int t = w * 64 + fresh_lane();
        int col;
        if (t < 576) {
          const int sec = t / 144, ch = t - 144 * sec;
          col = (sec + 2) * D + 4 * ch;
          mdst = L_MV + sec * 2304 + (16 * (ch >> 4) + ((ch >> 2) & 3)) * 16 + (ch & 3) * 4 + (64 << 20) + (sec == 2 ? (1 << 30) : 0);
        } else {
          const int k = t - 576, sec = k >= 36, ch = k - 36 * sec;
          col = sec * D + 144 * q + 4 * ch;
          mdst = L_SA + sec * 576 + 16 * ch + (4 << 20) + (sec ? (1 << 30) : 0);
          if (t >= 648) mdst = -1;
        }
        const int vo = t < 648 ? col * 4 : OOB;
        mp0 = cld128(rWs, vo, MODP_OFF);
        mp1 = cld128(rWs, vo, MODP_OFF + 6 * D * 4);
      }
      SCHED();
    }
    if (ds == 5) {
      if (mdst >= 0) {
        const int a0 = mdst & 0xfffff, stp = (mdst >> 20) & 0xff;
#pragma unroll
        for (int k = 0; k < 4; ++k) {
          float v = rbf(__uint_as_float(mp0[k]) + __uint_as_float(mp1[k]));
          if (mdst >> 30) v = rbf(1.f + v);
          *(LAS float*)(L + a0 + k * stp) = v;
        }
      }
      SCHED();
    }
#pragma unroll
    for (int kk = 0; kk < 2; ++kk) {
      u32x4 Wf[4];
#pragma unroll
      for (int rb = 0; rb < 4; ++rb) Wf[rb] = *(LAS u32x4*)(L + (ds & 3) * 32768 + rb * 2048 + (rd0 ^ (kk * 64)));
      SCHED();
#pragma unroll
      for (int j = 0; j < 3; ++j) {
        const int it = (2 * ds + kk) * 3 + j;
        const u32x4 a = R[it % 18];
#pragma unroll
        for (int rb = 0; rb < 4; ++rb) mac(acc[rb][j], a, Wf[rb], it < 3);
        SCHED();
        R[it % 18] = ld1(it + 18);
        SCHED();
      }
      if (cg == 0) {
#pragma unroll
        for (int rb = 0; rb < 4; ++rb)
#pragma unroll
          for (int e = 0; e < 4; ++e) {
            const unsigned we = Wf[rb][e];
            rs[rb] = __builtin_amdgcn_fdot2_f32_bf16(__builtin_bit_cast(bf16x2, we), __builtin_bit_cast(bf16x2, 0x3f803f80u), rs[rb], false);
          }
      }
      SCHED();
    }
  }
  MFMA_DONE();
  STAMP(8);
  f32x4 own[3];
  u32x4 R2[9];
  {
  const int lane = fresh_lane(), li = lane & 15, lg = lane >> 4;
#pragma unroll
  for (int rb = 0; rb < 4; ++rb) {
    if (rb != kq) {
      LAS char* p = L + ((cg * 4 + rb) * 3 + ((kq - rb - 1) & 3)) * 1536;
      *(LAS u32x4*)(p + vl16) = pk8(acc[rb][0], acc[rb][1]);
      *(LAS u32x2*)(p + 1024 + lane * 8) = pk4(acc[rb][2]);
    } else {
#pragma unroll
      for (int j = 0; j < 3; ++j) own[j] = acc[rb][j];
    }
    if (cg == 0) *(LAS float*)(L + L_RS + rb * 1024 + li * 64 + kq * 16 + lg * 4) = rs[rb];
  }
  }
  SCHED();
#pragma unroll
  for (int i = 0; i < 9; ++i) R2[i] = ld1(66 + i);
  BAR();
  {
    const int lane = fresh_lane(), li = lane & 15, lg = lane >> 4;
    LAS char* p = L + (cg * 4 + kq) * 4608;
    float rsum = 0.f;
#pragma unroll
    for (int k = 0; k < 4; ++k) {
      const f32x4 v = *(LAS f32x4*)(L + L_RS + kq * 1024 + li * 64 + k * 16);
      rsum += (v[0] + v[1]) + (v[2] + v[3]);
    }
#pragma unroll
    for (int s = 0; s < 3; ++s) {
      const u32x4 a = *(LAS u32x4*)(p + s * 1536 + vl16);
      const u32x2 c = *(LAS u32x2*)(p + s * 1536 + 1024 + lane * 8);
      own[0] += up4(a[0], a[1]);
      own[1] += up4(a[2], a[3]);
      own[2] += up4(c[0], c[1]);
    }
    const float sg = (kq & 2) ? -1.f : 1.f;
#pragma unroll
    for (int j = 0; j < 3; ++j) {
      const f32x4 sa = *(LAS f32x4*)(L + L_SA + (48 * cg + 16 * j + 4 * lg) * 4), ca = *(LAS f32x4*)(L + L_SA + 576 + (48 * cg + 16 * j + 4 * lg) * 4);
      *(LAS u32x2*)(L + L_PQ + ((kq & 1) * 9 + 3 * cg + j) * 1024 + vl16 + (kq >> 1) * 8) = pk4((own[j] * ca + rsum * sa) * sg);
    }
  }
  BAR();
  STAMP(9);
  {
    f32x4 ya[2][3];
#pragma unroll
    for (int t = 0; t < 2; ++t)
#pragma unroll
      for (int j = 0; j < 3; ++j) ya[t][j] = f32x4{0.f, 0.f, 0.f, 0.f};
#pragma unroll
    for (int kb = 0; kb < 9; ++kb) {
      const u32x4 B0 = *(LAS u32x4*)(L + L_PQ + kb * 1024 + vl16), B1 = *(LAS u32x4*)(L + L_PQ + (9 + kb) * 1024 + vl16);
#pragma unroll
      for (int j = 0; j < 3; ++j) {
        const int i = 3 * kb + j;
        const u32x4 a = i < 18 ? R[(12 + i) % 18] : R2[i < 18 ? 0 : i - 18];
        ya[0][j] = mfma(a, B0, ya[0][j]);
        ya[1][j] = mfma(a, B1, ya[1][j]);
      }
    }
    STAMP(10);
    const int lane = fresh_lane(), vpub = ((lane >> 2) & 3) * 36864 + (lane >> 4) * 64 + (lane & 3) * 16;
    const int yb = YP_OFF + (tb32 * 16 + q) * 9216 + w * 768;
    cst128(pk8(ya[0][0], ya[0][1]), rWs, vpub, yb);
    cst128(pk8(ya[1][0], ya[1][1]), rWs, vpub, yb + 256);
    cst128(pk8(ya[0][2], ya[1][2]), rWs, vpub, yb + 512);
  }
  VMW(0);
  BAR();
  STAMP(11);
  const int b1 = W1_OFF + (sl * 12 + w) * 55296, b2 = W2_OFF + (sl * 12 + w) * 55296;
  auto ld2 = [&](int it) -> u32x4 { return it < 54 ? cld128(rWs, vl16, b1 + it * 1024) : cld128(rWs, vl16, b2 + (it - 54) * 1024); };
  auto fin_map = [&](int& vlo, int& vhi, int& elo, int& ehi, int& hlo, int& hhi) {
    const int t = w * 64 + fresh_lane();
    const int pw = (t * 1366) >> 16, s = (t - 48 * pw) >> 4, g = (t >> 2) & 3, u = t & 3;
    const int f0 = 3 * pw + (pw & 1), f2 = 3 * pw + ((pw & 1) ? 0 : 2);
    const int flo = s < 2 ? f0 : f2, fhi = s < 2 ? f0 + 1 : f2, alo = s == 1, ahi = s >= 1;
    vlo = (flo * 4 + g) * 16;
    vhi = (fhi * 4 + g) * 16;
    elo = (4 * alo + u) * 1152 + (64 * (flo >> 2) + 16 * g + (flo & 3)) * 2;
    ehi = (4 * ahi + u) * 1152 + (64 * (fhi >> 2) + 16 * g + (fhi & 3)) * 2;
    hlo = ((flo >> 1) * 32 + alo * 16 + g * 4 + u) * 16 + (flo & 1) * 8;
    hhi = ((fhi >> 1) * 32 + ahi * 16 + g * 4 + u) * 16 + (fhi & 1) * 8;
  };
  if (w < 9) {
#pragma unroll
    for (int i = 0; i < 9; ++i) R[i] = ld2(i);
    SCHED();
    const int lane = fresh_lane(), tid = w * 64 + lane;
    int vlo, vhi, elo, ehi, hlo, hhi;
    fin_map(vlo, vhi, elo, ehi, hlo, hhi);
    const bool alo = (hlo >> 8) & 1, ahi = (hhi >> 8) & 1;
    f32x4 xl, xh;
#pragma unroll
    for (int r = 0; r < 4; ++r) {
      xl[r] = bflo(*(LAS u16*)(L + L_X + elo + r * 8));
      xh[r] = bflo(*(LAS u16*)(L + L_X + ehi + r * 8));
    }
    const f32x4 gal = *(LAS f32x4*)(L + L_MV + vlo), gah = *(LAS f32x4*)(L + L_MV + vhi);
    BAR();
    STAMP(12);
    u32x4 yp[4];
#pragma unroll
    for (int p = 0; p < 4; ++p) yp[p] = cld128(rWs, tid * 16, YP_OFF + ((tb32 * 4 + q) * 4 + p) * 9216);
    const f32x4 sbl = *(LAS f32x4*)(L + L_MV + 2304 + vlo), sbh = *(LAS f32x4*)(L + L_MV + 2304 + vhi);
    const f32x4 cbl = *(LAS f32x4*)(L + L_MV + 4608 + vlo), cbh = *(LAS f32x4*)(L + L_MV + 4608 + vhi);
    f32x4 yl = (up4(yp[0][0], yp[0][1]) + up4(yp[1][0], yp[1][1])) + (up4(yp[2][0], yp[2][1]) + up4(yp[3][0], yp[3][1]));
    f32x4 yh = (up4(yp[0][2], yp[0][3]) + up4(yp[1][2], yp[1][3])) + (up4(yp[2][2], yp[2][3]) + up4(yp[3][2], yp[3][3]));
    const u32x4 x2 = pk8(xl + gal * yl, xh + gah * yh);
    *(LAS u32x4*)(L + L_X + tid * 16) = x2;
    yl = up4(x2[0], x2[1]);
    yh = up4(x2[2], x2[3]);
    auto rowstat = [&](float lo, float hi, int buf, float& rl, float& rh) {
      const float s0 = sum16((alo ? 0.f : lo) + (ahi ? 0.f : hi)), s1 = sum16((alo ? lo : 0.f) + (ahi ? hi : 0.f));
      if (lane < 4) *(LAS f32x2*)(L + L_ST + buf * 288 + (w * 4 + lane) * 8) = f32x2{s0, s1};
      BAR();
      f32x2 t = {0.f, 0.f};
#pragma unroll
      for (int k = 0; k < 9; ++k) t += *(LAS f32x2*)(L + L_ST + buf * 288 + (k * 4 + (lane & 3)) * 8);
      rl = (alo ? t[1] : t[0]) * (1.f / D);
      rh = (ahi ? t[1] : t[0]) * (1.f / D);
    };
    float ml, mh, vl, vh;
    rowstat((yl[0] + yl[1]) + (yl[2] + yl[3]), (yh[0] + yh[1]) + (yh[2] + yh[3]), 0, ml, mh);
    yl -= ml;
    yh -= mh;
    const f32x4 ql = yl * yl, qh = yh * yh;
    rowstat((ql[0] + ql[1]) + (ql[2] + ql[3]), (qh[0] + qh[1]) + (qh[2] + qh[3]), 1, vl, vh);
    vl = rsqrtf(vl + 1e-5f);
    vh = rsqrtf(vh + 1e-5f);
    *(LAS u32x2*)(L + L_HS + hlo) = pk4(yl * vl * cbl + sbl);
    *(LAS u32x2*)(L + L_HS + hhi) = pk4(yh * vh * cbh + sbh);
    BAR();
    STAMP(13);
#pragma unroll
    for (int i = 9; i < 18; ++i) R[i] = ld2(i);
    SCHED();
    BAR();
  } else if (w == 9) {
    const int lane = fresh_lane();
    if (lane == 0) fst(ws + F3_OFF + (tb32 * 4 + q) * 128, tag);
    poll(ws + F3_OFF + (tb32 * 4 + (lane & 3)) * 128, tag);
    BAR();
    STAMP(12);
#pragma unroll
    for (int i = 0; i < 18; ++i) R[i] = ld2(i);
    SCHED();
    BAR();
    BAR();
    BAR();
    STAMP(13);
    u32x4 h[9];
#pragma unroll
    for (int i = 0; i < 9; ++i) h[i] = *(LAS u32x4*)(L + L_HS + i * 1024 + vl16);
#pragma unroll
    for (int i = 0; i < 9; ++i) cst128(h[i], rWs, vl16, H2_OFF + (rt * 8 + sl) * 9216 + i * 1024);
    VMW(0);
    if (lane == 0) fst(ws + F4_OFF + (rt * 8 + sl) * 128, tag);
    poll(ws + F4_OFF + (rt * 8 + (lane & 7)) * 128, tag);
    BAR();
  } else {
#pragma unroll
    for (int i = 0; i < 9; ++i) R[i] = ld2(i);
    SCHED();
    const int lane = fresh_lane(), li = lane & 15, lg = lane >> 4;
    const rsrc_t rW = mkrs(w2, job ? D * F * 2 : 0u);
    Unit u[3];
    int kb[3], g[3];
#pragma unroll
    for (int i = 0; i < 3; ++i) {
      const int uu = 6 * jr + 3 * (w - 10) + i;
      kb[i] = uu / 9;
      g[i] = uu - 9 * kb[i];
      unit_load(u[i], rW, lg * (16 * D * 2) + li * 8, jsl * (576 * D * 2) + g[i] * 128, D * 2, kb[i]);
    }
    BAR();
    STAMP(12);
    BAR();
    BAR();
    BAR();
    STAMP(13);
    const int vst = job ? vl16 : OOB;
#pragma unroll
    for (int i = 0; i < 3; ++i) unit_store(u[i], rWs, vst, W2_OFF + jsl * (12 * 54 * 1024) + kb[i] * 3072, g[i]);
#pragma unroll
    for (int i = 9; i < 18; ++i) R[i] = ld2(i);
    SCHED();
    BAR();
  }
  STAMP(14);
  {
    u32x4 h[6];
#pragma unroll
    for (int i = 0; i < 6; ++i) h[i] = cld128(rWs, vl16, H2_OFF + rt * 73728 + (i * 12 + w) * 1024);
    SCHED();
    const int lane = fresh_lane(), lg = lane >> 4;
    const int hv = ((lane >> 5) * 4 + (lg & 1)) * 1024 + ((lane >> 2) & 3) * 256 + (lane & 3) * 16;
#pragma unroll
    for (int i = 0; i < 6; ++i) {
      const int cw = i * 12 + w, j = cw / 9, kp = cw - 9 * j;
      *(LAS u32x4*)(L + (8 * kp + 2 * (j >> 2)) * 1024 + (j & 3) * 64 + hv) = h[i];
    }
    VMW(0);
  }
  BAR();
  STAMP(15);
  if (w == 10 && job && vl16 == 0) fst(ws + F6_OFF + b * 8, tag);
  const int fp = (3 * w + 1) >> 1, fh = (3 * (w & ~1) + 2) >> 1;
  u64 f6v = 0;
  auto f6addr = [&]() {
    const int lane = fresh_lane();
    return ws + F6_OFF + (27 * sl + (lane < 27 ? lane : 0)) * 8;
  };
#pragma unroll
  for (int ph = 0; ph < 2; ++ph) {
    LAS char* A = L + ph * L_H1 + vl16;
#pragma unroll
    for (int kb = 0; kb < 18; ++kb) {
      if (ph == 0 && kb == 6) f6v = fld(f6addr());
      if (ph == 0 && kb == 12) {
        if (__builtin_amdgcn_ballot_w64(f6v != tag) != 0) poll(f6addr(), tag);
        asm volatile("" ::: "memory");
      }
      u32x4 Bf[4];
#pragma unroll
      for (int mb = 0; mb < 4; ++mb) Bf[mb] = *(LAS u32x4*)(A + (kb * 4 + mb) * 1024);
      SCHED();
#pragma unroll
      for (int j = 0; j < 3; ++j) {
        const int it = ph * 54 + kb * 3 + j;
        const u32x4 a = R[it % 18];
#pragma unroll
        for (int mb = 0; mb < 4; ++mb) mac(acc[mb][j], a, Bf[mb], kb == 0);
        SCHED();
        if (it + 18 < 108) R[it % 18] = ld2(it + 18);
        SCHED();
      }
    }
    MFMA_DONE();
    STAMP(16 + 2 * ph);
    if (ph == 0) {
      LAS char* H = L + L_H1 + vl16;
#pragma unroll
      for (int mb = 0; mb < 4; ++mb) {
        *(LAS u32x4*)(H + (fp * 4 + mb) * 1024) = pk8(relu4(acc[mb][0]), relu4(acc[mb][1]));
        *(LAS u32x2*)(H + (fh * 4 + mb) * 1024 + (w & 1) * 8) = pk4(relu4(acc[mb][2]));
      }
      BAR();
      STAMP(17);
    }
  }
  {
    const int lane = fresh_lane(), vp2 = ((lane >> 2) & 3) * 73728 + (lane >> 4) * 64 + (lane & 3) * 16;
#pragma unroll
    for (int h = 0; h < 2; ++h) {
      const int ob = OP_OFF + ((rt * 8 + 4 * h) * 8 + sl) * 9216 + w * 768;
      cst128(pk8(acc[2 * h][0], acc[2 * h][1]), rWs, vp2, ob);
      cst128(pk8(acc[2 * h + 1][0], acc[2 * h + 1][1]), rWs, vp2, ob + 256);
      cst128(pk8(acc[2 * h][2], acc[2 * h + 1][2]), rWs, vp2, ob + 512);
    }
  }
  VMW(0);
  BAR();
  STAMP(19);
  if (w < 9) {
    const int tid = w * 64 + fresh_lane();
    int vlo, vhi, elo, ehi, hlo, hhi;
    fin_map(vlo, vhi, elo, ehi, hlo, hhi);
    const u32x4 x2 = *(LAS u32x4*)(L + L_X + tid * 16);
    const f32x4 gl = *(LAS f32x4*)(L + L_GB + vlo), gh = *(LAS f32x4*)(L + L_GB + vhi);
    BAR();
    STAMP(20);
    u32x4 p[8];
#pragma unroll
    for (int k = 0; k < 8; ++k) p[k] = cld128(rWs, tid * 16, OP_OFF + ((rt * 8 + sl) * 8 + k) * 9216);
    f32x4 sl4 = {0.f, 0.f, 0.f, 0.f}, sh4 = {0.f, 0.f, 0.f, 0.f};
#pragma unroll
    for (int k = 0; k < 8; ++k) {
      sl4 += up4(p[k][0], p[k][1]);
      sh4 += up4(p[k][2], p[k][3]);
    }
    const u32x4 o = pk8(up4(x2[0], x2[1]) + gl * sl4, up4(x2[2], x2[3]) + gh * sh4);
#pragma unroll
    for (int r = 0; r < 4; ++r) {
      *(LAS u16*)(L + elo + r * 8) = (u16)(o[r >> 1] >> (16 * (r & 1)));
      *(LAS u16*)(L + ehi + r * 8) = (u16)(o[2 + (r >> 1)] >> (16 * (r & 1)));
    }
    BAR();
    const int rr = (tid * 911) >> 16;
    *(u32x4*)((char*)out + (size_t)((32 * tb32 + 4 * q + 16 * (rr >> 2) + (rr & 3)) * 1152 + (tid - 72 * rr) * 16)) = *(LAS u32x4*)(L + tid * 16);
  } else {
    if (w == 9) {
      const int lane = fresh_lane();
      if (lane == 0) fst(ws + F5_OFF + (rt * 8 + sl) * 128, tag);
      poll(ws + F5_OFF + (rt * 8 + (lane & 7)) * 128, tag);
    }
    BAR();
    STAMP(20);
    BAR();
  }
  STAMP(21);
}

extern "C" void kernel_launch(void* const* d_in, const int* in_sizes, int n_in, void* d_out, int out_size, void* d_ws, size_t ws_size,
                              hipStream_t stream) {
  (void)hipFuncSetAttribute((const void*)fused_block, hipFuncAttributeMaxDynamicSharedMemorySize, LDS_BYTES);
  fused_block<<<256, 768, LDS_BYTES, stream>>>((const u16*)d_in[0], (const u16*)d_in[1], (const u16*)d_in[2], (const u16*)d_in[3],
                                               (const u16*)d_in[4], (const u16*)d_in[6], (const u16*)d_in[7], (const u16*)d_in[8],
                                               (const u16*)d_in[9], (char*)d_ws, (u16*)d_out);
}
```

```cpp
#include <hip/hip_runtime.h>
#include <hip/hip_bf16.h>
#include <stdint.h>

typedef unsigned short u16;
typedef unsigned long long u64;
typedef __attribute__((ext_vector_type(2))) float f32x2;
typedef __attribute__((ext_vector_type(4))) float f32x4;
typedef __attribute__((ext_vector_type(2))) __bf16 bf16x2;
typedef __attribute__((ext_vector_type(8))) __bf16 bf16x8;
typedef __attribute__((ext_vector_type(4))) short s16x4;
typedef __attribute__((ext_vector_type(2))) unsigned u32x2;
typedef __attribute__((ext_vector_type(4))) unsigned u32x4;
typedef __amdgpu_buffer_rsrc_t rsrc_t;
#define LAS __attribute__((address_space(3)))
typedef __attribute__((address_space(1))) u64 gu64;

constexpr int S = 2048, D = 576, F = 4608;
#define ROTQ 2
#define ROTX 0
constexpr unsigned E_OFF = 0, F1_OFF = 0x1000  , F2_OFF = 0x10000, F3_OFF = 0x20000, F4_OFF = 0x28000, F5_OFF = 0x30000, F6_OFF = 0x38000;
constexpr unsigned MODP_OFF = 0x40000;
constexpr unsigned XN_OFF = 0x80000;
constexpr unsigned WD_OFF = XN_OFF + S * D * 2;
constexpr unsigned W1_OFF = WD_OFF + D * D * 4;
constexpr unsigned W2_OFF = W1_OFF + D * F * 2;
constexpr unsigned YP_OFF = W2_OFF + D * F * 2;
constexpr unsigned H2_OFF = YP_OFF + 1024 * 9216;
constexpr unsigned OP_OFF = H2_OFF + 256 * 9216;
constexpr unsigned WS_END = OP_OFF + 2048 * 9216;
constexpr int SC1 = 16;
constexpr int OOB = (int)0x80000000u;
constexpr int NP = 216;
constexpr int L_RAW = 0, L_PQ = 49152, L_RED = 67584, L_HS = 98304, L_H1 = 73728;
constexpr int L_STG = 131072, STG_ROW = 1216;
constexpr int L_RS = 131072, L_SA = 135168, L_ST = 137216, L_MV = 140544, L_GB = 147456, L_X = 149760, L_CT = 158976, L_STAMP = 159232;
constexpr int LDS_BYTES = 163840;
constexpr int SPIN_CAP = 1 << 17;

__device__ __forceinline__ rsrc_t mkrs(const void* p, unsigned bytes) {
  return __builtin_amdgcn_make_buffer_rsrc((void*)p, 0, bytes, 0x00020000);
}
__device__ __forceinline__ u32x4 mksrd(const void* p, unsigned bytes) {
  const u64 a = (u64)p;
  return u32x4{(unsigned)a, (unsigned)(a >> 32) & 0xffffu, bytes, 0x00020000u};
}
#define NTL 2
__device__ __forceinline__ u32x4 bld128(rsrc_t r, int v, int s) { return __builtin_amdgcn_raw_buffer_load_b128(r, v, s, NTL); }
__device__ __forceinline__ u32x4 cld128(rsrc_t r, int v, int s) { return __builtin_amdgcn_raw_buffer_load_b128(r, v, s, SC1); }
__device__ __forceinline__ u32x2 bld64(rsrc_t r, int v, int s) { return __builtin_amdgcn_raw_buffer_load_b64(r, v, s, NTL); }
#define SCHED() __builtin_amdgcn_sched_barrier(0)
__device__ __forceinline__ void cst128(u32x4 d, rsrc_t r, int v, int s) {
  __builtin_amdgcn_raw_buffer_store_b128(d, r, v, s, SC1);
  SCHED();
  asm volatile("s_nop 1" ::: "memory");
  SCHED();
}
template <bool NT>
__device__ __forceinline__ void dma(unsigned lds, int voff, u32x4 srd, int soff) {
  if (NT) asm volatile("s_mov_b32 m0, %0\n\ts_nop 3\n\tbuffer_load_dwordx4 %1, %2, %3 offen nt lds" ::"s"(lds), "v"(voff), "s"(srd), "s"(soff) : "memory");
  else asm volatile("s_mov_b32 m0, %0\n\ts_nop 3\n\tbuffer_load_dwordx4 %1, %2, %3 offen lds" ::"s"(lds), "v"(voff), "s"(srd), "s"(soff) : "memory");
}
#define NT2 false
__device__ __forceinline__ void refill(u32x4& r, int voff, u32x4 srd, int soff) {
  asm volatile("buffer_load_dwordx4 %0, %1, %2, %3 offen sc1" : "+v"(r) : "v"(voff), "s"(srd), "s"(soff) : "memory");
}
__device__ __forceinline__ void fill(u32x4& r, int voff, u32x4 srd, int soff) {
  asm volatile("buffer_load_dwordx4 %0, %1, %2, %3 offen sc1" : "=v"(r) : "v"(voff), "s"(srd), "s"(soff) : "memory");
}
#define BAR() asm volatile("s_waitcnt lgkmcnt(0)\n\ts_barrier" ::: "memory")
#define VMW(n) asm volatile("s_waitcnt vmcnt(" #n ")" ::: "memory")
#define PIN(x) asm volatile("" : "+v"(x))
__device__ __forceinline__ int fresh_lane() {
  int l;
  asm volatile("v_mbcnt_lo_u32_b32 %0, -1, 0\n\tv_mbcnt_hi_u32_b32 %0, -1, %0" : "=v"(l));
  return l;
}
#define LANES() const int lane = fresh_lane(), li = lane & 15, lg = lane >> 4, vl16 = lane * 16, tid = w * 64 + lane; (void)li, (void)lg, (void)vl16, (void)tid
__device__ __forceinline__ float bflo(unsigned u) { return __uint_as_float(u << 16); }
__device__ __forceinline__ float bfhi(unsigned u) { return __uint_as_float(u & 0xffff0000u); }
__device__ __forceinline__ unsigned pk(float a, float b) {
  f32x2 v = {a, b};
  return __builtin_bit_cast(unsigned, __builtin_convertvector(v, bf16x2));
}
__device__ __forceinline__ float rbf(float a) { return bflo(pk(a, 0.f)); }
__device__ __forceinline__ u32x2 pk4(f32x4 a) { return u32x2{pk(a[0], a[1]), pk(a[2], a[3])}; }
__device__ __forceinline__ u32x4 pk8(f32x4 a, f32x4 b) { return u32x4{pk(a[0], a[1]), pk(a[2], a[3]), pk(b[0], b[1]), pk(b[2], b[3])}; }
__device__ __forceinline__ f32x4 up4(unsigned a, unsigned b) { return f32x4{bflo(a), bfhi(a), bflo(b), bfhi(b)}; }
__device__ __forceinline__ f32x4 relu4(f32x4 a) { return f32x4{fmaxf(a[0], 0.f), fmaxf(a[1], 0.f), fmaxf(a[2], 0.f), fmaxf(a[3], 0.f)}; }
__device__ __forceinline__ f32x4 mfma(u32x4 a, u32x4 b, f32x4 c) {
  return __builtin_amdgcn_mfma_f32_16x16x32_bf16(__builtin_bit_cast(bf16x8, a), __builtin_bit_cast(bf16x8, b), c, 0, 0, 0);
}
__device__ __forceinline__ void mac(f32x4& c, u32x4 a, u32x4 b, bool first) {
  if (first) asm("v_mfma_f32_16x16x32_bf16 %0, %1, %2, 0" : "=&v"(c) : "v"(a), "v"(b));
  else asm("v_mfma_f32_16x16x32_bf16 %0, %1, %2, %0" : "+v"(c) : "v"(a), "v"(b));
}
#define MFMA_DONE() do { SCHED(); asm volatile("s_nop 7"); SCHED(); } while (0)
template <int C>
__device__ __forceinline__ float dppadd(float v) {
  return v + __uint_as_float(__builtin_amdgcn_update_dpp(0, __float_as_uint(v), C, 0xf, 0xf, false));
}
__device__ __forceinline__ float sum16(float v) {
  v = dppadd<0x124>(v);
  v = dppadd<0x128>(v);
  auto a = __builtin_amdgcn_permlane16_swap(__float_as_uint(v), __float_as_uint(v), false, false);
  v = __uint_as_float(a[0]) + __uint_as_float(a[1]);
  auto c = __builtin_amdgcn_permlane32_swap(__float_as_uint(v), __float_as_uint(v), false, false);
  return __uint_as_float(c[0]) + __uint_as_float(c[1]);
}
__device__ __forceinline__ float wsum(float v) { return sum16(dppadd<0x4e>(dppadd<0xb1>(v))); }
__device__ __forceinline__ u64 fld(const char* p) { return __hip_atomic_load((gu64*)p, __ATOMIC_RELAXED, __HIP_MEMORY_SCOPE_AGENT); }
__device__ __forceinline__ void fst(char* p, u64 v) { __hip_atomic_store((gu64*)p, v, __ATOMIC_RELAXED, __HIP_MEMORY_SCOPE_AGENT); }
__device__ __forceinline__ void poll(const char* p, u64 tag) {
  for (int it = 0; it < SPIN_CAP; ++it) {
    const u64 v = fld(p);
    if (__builtin_amdgcn_ballot_w64(v != tag) == 0) break;
    __builtin_amdgcn_s_sleep(1);
  }
  asm volatile("" ::: "memory");
}
__device__ __forceinline__ int slot_of(int f) {
  const int w = (f * 43) >> 7, r = f - 3 * w;
  return w * 54 + ((w & 1) ? (r == 0 ? 2 : r - 1) : r);
}
__device__ __forceinline__ void lds_wait(LAS char* p, unsigned v) {
  for (int it = 0; it < SPIN_CAP * 8; ++it) {
    if (__hip_atomic_load((LAS unsigned*)p, __ATOMIC_RELAXED, __HIP_MEMORY_SCOPE_WORKGROUP) == v) break;
    __builtin_amdgcn_s_sleep(1);
  }
  asm volatile("" ::: "memory");
}
__device__ __forceinline__ unsigned lds_add(LAS char* p) {
  asm volatile("s_waitcnt lgkmcnt(0)" ::: "memory");
  return __hip_atomic_fetch_add((LAS unsigned*)p, 1u, __ATOMIC_RELAXED, __HIP_MEMORY_SCOPE_WORKGROUP);
}

#define STAMP(i)

__global__ __launch_bounds__(768, 3) void fused_block(const u16* __restrict__ x, const u16* __restrict__ wre, const u16* __restrict__ wim,
                                                      const u16* __restrict__ dre, const u16* __restrict__ dim, const u16* __restrict__ w1,
                                                      const u16* __restrict__ w2, const u16* __restrict__ cvec, const u16* __restrict__ wmod,
                                                      char* __restrict__ ws, u16* __restrict__ out) {
  extern __shared__ __attribute__((aligned(16))) char smem[];
  LAS char* L = (LAS char*)smem;
  const unsigned lb = (unsigned)(size_t)L;
  const int w = __builtin_amdgcn_readfirstlane(threadIdx.x >> 6);
  const int b = blockIdx.x, x8 = b & 7, idx = b >> 3;
  const int q = idx & 3, rt = 8 * (x8 >> 1) + (idx >> 2), hf = x8 & 1, tb32 = 2 * rt + hf, sl = 4 * hf + q;
  const int kq = w & 3, cg = w >> 2;
  const int rot = (ROTQ * q + ROTX * (idx >> 2)) & 7;
  const rsrc_t rWs = mkrs(ws, WS_END);
  const u32x4 sRe = mksrd(wre, S * S * 2), sIm = mksrd(wim, S * S * 2);
  auto mk_vdma = [&]() {
    const int lane = fresh_lane();
    return (lane >> 3) * 4096 + (((lane & 7) ^ ((4 * (w & 1) + (lane >> 4)) & 7)) * 16);
  };
  const int ws_row = 32 * tb32 * 4096;
  const bool job = b < NP;
  const int jsl = b / 27, ju = 6 * (b - 27 * jsl) + (w < 6 ? w : w - 6), jkb = ju / 9, jg = ju - 9 * jkb;
  STAMP(0);
  auto dma_slot = [&](int ds) {
    const int vdma = mk_vdma();
    const int so = ws_row + kq * 32768 + ((ds + rot) & 7) * 128;
#pragma unroll
    for (int k = 0; k < 4; ++k) {
      if (ds < 2) {
        dma<true>(lb + ds * 32768 + k * 8192 + kq * 1024, vdma, sRe, so + k * 1024);
        dma<true>(lb + ds * 32768 + k * 8192 + kq * 1024 + 4096, vdma, sIm, so + k * 1024);
      } else {
        dma<false>(lb + ds * 32768 + k * 8192 + kq * 1024, vdma, sRe, so + k * 1024);
        dma<false>(lb + ds * 32768 + k * 8192 + kq * 1024 + 4096, vdma, sIm, so + k * 1024);
      }
    }
  };
  u64 tag;
  auto get_tag = [&]() {
    const u32x2 t = *(LAS u32x2*)(L + L_CT);
    tag = ((u64)(unsigned)__builtin_amdgcn_readfirstlane(t.y) << 32) | (unsigned)__builtin_amdgcn_readfirstlane(t.x);
  };
  constexpr int C_LN = L_CT + 16, C_SL = L_CT + 20, C_ST = L_CT + 24, C_F1 = L_CT + 28, C_XI = L_CT + 32, C_H2 = L_CT + 36;
  if (w < 8) {
    LANES();
    const rsrc_t rX = mkrs(x, S * D * 2);
    const u32x4 v0 = bld128(rX, vl16, (8 * b + w) * 1152), v1 = bld128(rX, lane < 8 ? vl16 + 1024 : OOB, (8 * b + w) * 1152);
    const u32x2 e = __builtin_amdgcn_raw_buffer_load_b64(rWs, tid == 0 ? E_OFF : OOB, 0, SC1);
    SCHED();
    if (tid < 2) *(LAS u32x4*)(L + C_LN + vl16) = u32x4{0u, 0u, 0u, 0u};
    BAR();
    int so[9];
    if (w == 0) {
#pragma unroll
      for (int i = 0; i < 9; ++i) {
        const int db = 4 * i + lg, d3 = (db * 43) >> 7;
        so[i] = d3 * 196608 + (db - 3 * d3) * 1024 + li * 16;
        PIN(so[i]);
      }
    }
    float f[16];
#pragma unroll
    for (int i = 0; i < 4; ++i) {
      f[2 * i] = bflo(v0[i]);
      f[2 * i + 1] = bfhi(v0[i]);
      f[8 + 2 * i] = bflo(v1[i]);
      f[9 + 2 * i] = bfhi(v1[i]);
    }
    float s = 0.f;
#pragma unroll
    for (int i = 0; i < 8; ++i) s += f[i] + f[8 + i];
    const float m = wsum(s) * (1.f / D);
    float qa = 0.f, qb = 0.f;
#pragma unroll
    for (int i = 0; i < 8; ++i) {
      f[i] -= m;
      f[8 + i] -= m;
      qa += f[i] * f[i];
      qb += f[8 + i] * f[8 + i];
    }
    const float r = rsqrtf(wsum(qa + (lane < 8 ? qb : 0.f)) * (1.f / D) + 1e-5f);
    if (tid == 0) *(LAS u64*)(L + L_CT) = __builtin_bit_cast(u64, e) + 1;
    LAS char* st = L + L_STG + w * STG_ROW + vl16;
    *(LAS u32x4*)st = u32x4{pk(f[0] * r, f[1] * r), pk(f[2] * r, f[3] * r), pk(f[4] * r, f[5] * r), pk(f[6] * r, f[7] * r)};
    if (lane < 8)
      *(LAS u32x4*)(st + 1024) = u32x4{pk(f[8] * r, f[9] * r), pk(f[10] * r, f[11] * r), pk(f[12] * r, f[13] * r), pk(f[14] * r, f[15] * r)};
    if (lane == 0) lds_add(L + C_LN);
    lds_wait(L + C_LN, 8);
    STAMP(1);
    get_tag();
    if (w == 0) {
      LAS char* rp = L + L_STG + (li >> 2) * STG_ROW + lg * 32 + (lane & 3) * 8;
      u32x4 o[9];
#pragma unroll
      for (int i = 0; i < 9; ++i) {
        const u32x2 lo = __builtin_bit_cast(u32x2, __builtin_amdgcn_ds_read_tr16_b64_v4i16((LAS s16x4*)(rp + i * 128)));
        const u32x2 hi = __builtin_bit_cast(u32x2, __builtin_amdgcn_ds_read_tr16_b64_v4i16((LAS s16x4*)(rp + i * 128 + 4 * STG_ROW)));
        o[i] = u32x4{lo.x, lo.y, hi.x, hi.y};
      }
      const int xb = XN_OFF + (b >> 2) * 3072 + (b & 3) * 256;
#pragma unroll
      for (int i = 0; i < 9; ++i) cst128(o[i], rWs, so[i], xb);
      if (lane == 0) lds_add(L + C_XI);
      VMW(0);
      if (lane < 8) fst(ws + F1_OFF + lane * 2048 + b * 8, tag);
    } else if (w == 1) {
      const unsigned tl = (unsigned)tag, th = (unsigned)(tag >> 32);
      for (int it = 0; it < SPIN_CAP; ++it) {
        const u32x4 p0 = cld128(rWs, vl16, F1_OFF + x8 * 2048), p1 = cld128(rWs, vl16, F1_OFF + x8 * 2048 + 1024);
        const unsigned d = ((p0.x ^ tl) | (p0.y ^ th)) | ((p0.z ^ tl) | (p0.w ^ th)) | ((p1.x ^ tl) | (p1.y ^ th)) | ((p1.z ^ tl) | (p1.w ^ th));
        if (__builtin_amdgcn_ballot_w64(d != 0) == 0) break;
        __builtin_amdgcn_s_sleep(1);
      }
      asm volatile("" ::: "memory");
      if (lane == 0) lds_add(L + C_F1);
    } else if (w >= 4) {
      lds_wait(L + C_SL, 4);
      dma_slot(2);
      dma_slot(3);
    }
  } else {
    BAR();
    dma_slot(0);
    lds_wait(L + C_XI, 1);
    dma_slot(1);
    LANES();
    if (w == 8) {
      const rsrc_t rM = mkrs(wmod, job ? D * 6 * D * 2 : 0u), rC = mkrs(cvec, D * 2);
      const int r0 = 18 * (lane >> 1), cgp = 27 * x8 + idx;
      const u32x4 c0 = bld128(rC, r0 * 2, 0), c1 = bld128(rC, r0 * 2, 16);
      const unsigned c2 = __builtin_amdgcn_raw_buffer_load_b32(rC, r0 * 2, 32, 0);
      u32x4 mv[18];
#pragma unroll
      for (int i = 0; i < 18; ++i) mv[i] = bld128(rM, r0 * (6 * D * 2) + (lane & 1) * 16, cgp * 32 + i * (6 * D * 2));
      VMW(21);
      STAMP(22);
      if (lane == 0) lds_add(L + C_SL);

      float a[8];
#pragma unroll
      for (int j = 0; j < 8; ++j) a[j] = 0.f;
#pragma unroll
      for (int i = 0; i < 18; ++i) {
        const unsigned cw = i < 8 ? c0[(i >> 1) & 3] : (i < 16 ? c1[(i >> 1) & 3] : c2);
        const float cv = (i & 1) ? bfhi(cw) : bflo(cw);
        const float sv = rbf(cv / (1.f + __expf(-cv)));
#pragma unroll
        for (int j = 0; j < 4; ++j) {
          a[2 * j] += sv * bflo(mv[i][j]);
          a[2 * j + 1] += sv * bfhi(mv[i][j]);
        }
      }
      const bool one = (cgp >= 36 && cgp < 72) || (cgp >= 144 && cgp < 180);
#pragma unroll
      for (int j = 0; j < 8; ++j) {
        a[j] = rbf(sum16(dppadd<0x4e>(a[j])));
        if (one) a[j] = rbf(1.f + a[j]);
      }
      const f32x4 o = (lane & 2) ? f32x4{a[4], a[5], a[6], a[7]} : f32x4{a[0], a[1], a[2], a[3]};
      cst128(__builtin_bit_cast(u32x4, o), rWs, (lane < 4 && job) ? (lane & 1) * 32 + (lane & 2) * 8 : OOB, MODP_OFF + cgp * 64);
    } else {
      const rsrc_t rDr = mkrs(dre, job ? D * D * 2 : 0u), rDi = mkrs(dim, job ? D * D * 2 : 0u);
      u32x2 da[2], dbv[2];
      const int fr = 6 * b + 2 * (w - 9), ff = fr / 36, db = fr - 36 * ff;
#pragma unroll
      for (int i = 0; i < 2; ++i) {
        const int so = (32 * (ff >> 1) + 4 * (ff & 1)) * 1152 + (db + i) * 32, vo = (8 * (li >> 2) + (li & 3)) * 1152 + lg * 8;
        da[i] = bld64(rDr, vo, so);
        dbv[i] = bld64(rDi, vo, so);
      }
      VMW(4);
      STAMP(22);
      if (lane == 0) lds_add(L + C_SL);
      const int sf = slot_of(ff), fw = sf / 54, fl = sf - 54 * fw;
#pragma unroll
      for (int i = 0; i < 2; ++i) {
        const int dq = (db + i) / 9, dk = db + i - 9 * dq;
        cst128(u32x4{da[i].x, da[i].y, dbv[i].x, dbv[i].y}, rWs, job ? vl16 : OOB, WD_OFF + (((dq * 12 + fw) * 9 + dk) * 3 + fl) * 1024);
      }
    }
    get_tag();
    VMW(0);
    if (lane == 0) {
      if (lds_add(L + C_ST) == 3) fst(ws + F2_OFF + b * 8, tag);
    }
    STAMP(1);
  }
  STAMP(2);
  lds_wait(L + C_F1, 1);
  STAMP(3);
  const int xbase = XN_OFF + ((q * 3 + cg) * 4 + kq) * 49152;
  const int dbase = WD_OFF + (q * 12 + w) * 27648;
  u32x4 R[18];
  int vl16, rd0;
  {
    const int lane = fresh_lane(), li = lane & 15, lg = lane >> 4;
    vl16 = lane * 16;
    rd0 = kq * 8192 + li * 128 + ((lg ^ (li >> 1)) * 16);
  }
  auto ld1 = [&](int it) -> u32x4 {
    if (it < 48) {
      const int ks = it / 3, j = it - 3 * ks;
      return cld128(rWs, vl16, xbase + ((((ks >> 1) + rot) & 7) * 2 + (ks & 1)) * 3072 + j * 1024);
    }
    return cld128(rWs, vl16, dbase + (it - 48) * 1024);
  };
#pragma unroll
  for (int i = 0; i < 18; ++i) R[i] = ld1(i);
  SCHED();
  lds_wait(L + C_SL, 4);
  STAMP(4);
  f32x4 acc[4][3];
  float rs[4];
#pragma unroll
  for (int r = 0; r < 4; ++r) rs[r] = 0.f;
  u64 f2v = 0;
  auto f2addr = [&]() {
    const int lane = fresh_lane();
    return ws + F2_OFF + (54 * kq + (lane < 54 ? lane : 0)) * 8;
  };
  u32x4 mp0;
  int mdst = 0;
#pragma unroll
  for (int ds = 0; ds < 8; ++ds) {
    if (ds == 2 || ds == 4 || ds == 6) {
      if (ds == 4 && w >= 8) {
        if (__builtin_amdgcn_ballot_w64(f2v != tag) != 0) poll(f2addr(), tag);
      }
      if (ds > 2) VMW(12);
      BAR();
      STAMP(4 + (ds >> 1));
      if (ds < 6 && w < 8) {
        const u32x4 sW = w < 4 ? sRe : sIm;
        const int vdma = mk_vdma();
#pragma unroll
        for (int d = 0; d < 2; ++d) {
          const int so = ws_row + kq * 32768 + ((ds + 2 + d + rot) & 7) * 128;
#pragma unroll
          for (int k = 0; k < 4; ++k) dma<NT2>(lb + ((ds + 2 + d) & 3) * 32768 + k * 8192 + w * 1024, vdma, sW, so + k * 1024);
        }
      }
      if (ds == 6) {
        const int lane = fresh_lane();
        if (w < 9) {
          const int ch = w * 64 + lane, rr = (ch * 911) >> 16;
          dma<true>(lb + L_X + w * 1024, (16 * (rr >> 2) + (rr & 3)) * 1152 + (ch - 72 * rr) * 16, mksrd(x, S * D * 2), (32 * tb32 + 4 * q) * 1152);
        }
        const int st = w < 6 ? F * 2 : D * 2;
        const u32x4 sW = mksrd(w < 6 ? w1 : w2, job ? D * F * 2 : 0u);
        const int so = w < 6 ? jsl * 1152 + jg * 128 + jkb * (32 * F * 2) : (jsl * 576 + jkb * 32) * (D * 2) + jg * 128;
#pragma unroll
        for (int k = 0; k < 4; ++k) dma<true>(lb + L_RAW + w * 4096 + k * 1024, (lane >> 3) * st + (lane & 7) * 16, sW, so + k * 8 * st);
      }
      if (ds == 2) {
        if (w >= 8) f2v = fld(f2addr());
        if (b == 0 && w == 0 && vl16 == 0) fst(ws + E_OFF, tag);
      }
      if (ds == 4) {
        const int t = w * 64 + fresh_lane(), k = t - 576;
        mdst = t < 576 ? L_MV + t * 16 : L_SA + k * 16;
        mp0 = cld128(rWs, t < 576 ? 2 * D * 4 + t * 16 : (k < 36 ? k * 16 + q * 576 : (k < 72 ? D * 4 + (k - 36) * 16 + q * 576 : OOB)), MODP_OFF);
      }
      SCHED();
    }
    if (ds == 5) {
      if (w < 11) *(LAS u32x4*)(L + mdst) = mp0;
      SCHED();
    }
#pragma unroll
    for (int kk = 0; kk < 2; ++kk) {
      u32x4 Wf[4];
#pragma unroll
      for (int rb = 0; rb < 4; ++rb) Wf[rb] = *(LAS u32x4*)(L + (ds & 3) * 32768 + rb * 2048 + (rd0 ^ (kk * 64)));
      SCHED();
#pragma unroll
      for (int j = 0; j < 3; ++j) {
        const int it = (2 * ds + kk) * 3 + j;
        const u32x4 a = R[it % 18];
#pragma unroll
        for (int rb = 0; rb < 4; ++rb) mac(acc[rb][j], a, Wf[rb], it < 3);
        SCHED();
        R[it % 18] = ld1(it + 18);
        SCHED();
      }
      if (cg == 0) {
#pragma unroll
        for (int rb = 0; rb < 4; ++rb)
#pragma unroll
          for (int e = 0; e < 4; ++e) {
            const unsigned we = Wf[rb][e];
            rs[rb] = __builtin_amdgcn_fdot2_f32_bf16(__builtin_bit_cast(bf16x2, we), __builtin_bit_cast(bf16x2, 0x3f803f80u), rs[rb], false);
          }
      }
      SCHED();
    }
  }
  MFMA_DONE();
  STAMP(8);
  BAR();
  f32x4 own[3];
  u32x4 R2[9];
  {
  const int lane = fresh_lane(), li = lane & 15, lg = lane >> 4;
#pragma unroll
  for (int rb = 0; rb < 4; ++rb) {
    if (rb != kq) {
      LAS char* p = L + L_RED + ((cg * 4 + rb) * 3 + ((kq - rb - 1) & 3)) * 1536;
      *(LAS u32x4*)(p + vl16) = pk8(acc[rb][0], acc[rb][1]);
      *(LAS u32x2*)(p + 1024 + lane * 8) = pk4(acc[rb][2]);
    } else {
#pragma unroll
      for (int j = 0; j < 3; ++j) own[j] = acc[rb][j];
    }
    if (cg == 0) *(LAS float*)(L + L_RS + rb * 1024 + li * 64 + kq * 16 + lg * 4) = rs[rb];
  }
  }
  SCHED();
#pragma unroll
  for (int i = 0; i < 9; ++i) R2[i] = ld1(66 + i);
  BAR();
  {
    const int lane = fresh_lane(), li = lane & 15, lg = lane >> 4;
    LAS char* p = L + L_RED + (cg * 4 + kq) * 4608;
    float rsum = 0.f;
#pragma unroll
    for (int k = 0; k < 4; ++k) {
      const f32x4 v = *(LAS f32x4*)(L + L_RS + kq * 1024 + li * 64 + k * 16);
      rsum += (v[0] + v[1]) + (v[2] + v[3]);
    }
#pragma unroll
    for (int s = 0; s < 3; ++s) {
      const u32x4 a = *(LAS u32x4*)(p + s * 1536 + vl16);
      const u32x2 c = *(LAS u32x2*)(p + s * 1536 + 1024 + lane * 8);
      own[0] += up4(a[0], a[1]);
      own[1] += up4(a[2], a[3]);
      own[2] += up4(c[0], c[1]);
    }
    const float sg = (kq & 2) ? -1.f : 1.f;
#pragma unroll
    for (int j = 0; j < 3; ++j) {
      const f32x4 sa = *(LAS f32x4*)(L + L_SA + (48 * cg + 16 * j + 4 * lg) * 4), ca = *(LAS f32x4*)(L + L_SA + 576 + (48 * cg + 16 * j + 4 * lg) * 4);
      *(LAS u32x2*)(L + L_PQ + ((kq & 1) * 9 + 3 * cg + j) * 1024 + vl16 + (kq >> 1) * 8) = pk4((own[j] * ca + rsum * sa) * sg);
    }
  }
  VMW(21);
  BAR();
  STAMP(9);
  {
    const int lane = fresh_lane();
    LAS char* rp = L + L_RAW + w * 4096 + ((lane >> 4) * 8 + ((lane >> 2) & 3)) * 128 + (lane & 3) * 16;
    const int base = (w < 6 ? W1_OFF : W2_OFF) + jsl * (12 * 54 * 1024) + jkb * 3072, vst = job ? vl16 : OOB;
#pragma unroll
    for (int cc = 0; cc < 4; ++cc) {
      const u32x2 lo = __builtin_bit_cast(u32x2, __builtin_amdgcn_ds_read_tr16_b64_v4i16((LAS s16x4*)(rp + (cc >> 1) * 64 + (cc & 1) * 8)));
      const u32x2 hi = __builtin_bit_cast(u32x2, __builtin_amdgcn_ds_read_tr16_b64_v4i16((LAS s16x4*)(rp + (cc >> 1) * 64 + (cc & 1) * 8 + 512)));
      cst128(u32x4{lo.x, lo.y, hi.x, hi.y}, rWs, vst, base + slot_of(4 * jg + cc) * 1024);
    }
  }
  {
    f32x4 ya[2][3];
#pragma unroll
    for (int t = 0; t < 2; ++t)
#pragma unroll
      for (int j = 0; j < 3; ++j) ya[t][j] = f32x4{0.f, 0.f, 0.f, 0.f};
#pragma unroll
    for (int kb = 0; kb < 9; ++kb) {
      const u32x4 B0 = *(LAS u32x4*)(L + L_PQ + kb * 1024 + vl16), B1 = *(LAS u32x4*)(L + L_PQ + (9 + kb) * 1024 + vl16);
#pragma unroll
      for (int j = 0; j < 3; ++j) {
        const int i = 3 * kb + j;
        const u32x4 a = i < 18 ? R[(12 + i) % 18] : R2[i < 18 ? 0 : i - 18];
        ya[0][j] = mfma(a, B0, ya[0][j]);
        ya[1][j] = mfma(a, B1, ya[1][j]);
      }
    }
    STAMP(10);
    const int lane = fresh_lane(), vpub = ((lane >> 2) & 3) * 36864 + (lane >> 4) * 64 + (lane & 3) * 16;
    const int yb = YP_OFF + (tb32 * 16 + q) * 9216 + w * 768;
    cst128(pk8(ya[0][0], ya[0][1]), rWs, vpub, yb);
    cst128(pk8(ya[1][0], ya[1][1]), rWs, vpub, yb + 256);
    cst128(pk8(ya[0][2], ya[1][2]), rWs, vpub, yb + 512);
  }
  VMW(0);
  BAR();
  STAMP(11);
  const int b1 = W1_OFF + (sl * 12 + w) * 55296, b2 = W2_OFF + (sl * 12 + w) * 55296;
  const u32x4 sWs = mksrd(ws, WS_END);
  auto ld2 = [&](int it) -> u32x4 { return it < 54 ? cld128(rWs, vl16, b1 + it * 1024) : cld128(rWs, vl16, b2 + (it - 54) * 1024); };
  auto fin_map = [&](int t, int& flo, int& fhi, bool& alo, bool& ahi) {
    const int pw = (t * 1366) >> 16, s = (t - 48 * pw) >> 4, g = (t >> 2) & 3;
    const int f2 = 3 * pw + ((pw & 1) ? 0 : 2);
    flo = 8 * g + (s < 2 ? 32 * ((3 * pw + 1) >> 1) : 32 * (f2 >> 1) + 4 * (f2 & 1));
    fhi = flo + (s < 2 ? 4 : 0);
    alo = s == 1;
    ahi = s >= 1;
  };
  auto gather_h2 = [&]() {
    STAMP(14);
    u32x4 h[6];
#pragma unroll
    for (int i = 0; i < 6; ++i) h[i] = cld128(rWs, vl16, H2_OFF + rt * 73728 + (i * 12 + w) * 1024);
    SCHED();
    const int lane = fresh_lane(), lg = lane >> 4;
    const int hv = ((lane >> 5) * 4 + (lg & 1)) * 1024 + ((lane >> 2) & 3) * 256 + (lane & 3) * 16;
#pragma unroll
    for (int i = 0; i < 6; ++i) {
      const int cw = i * 12 + w, j = cw / 9, kp = cw - 9 * j;
      *(LAS u32x4*)(L + (8 * kp + 2 * (j >> 2)) * 1024 + (j & 3) * 64 + hv) = h[i];
    }
    BAR();
    STAMP(15);
  };
  auto window = [&](int i0) {
#pragma unroll
    for (int i = 0; i < 9; ++i) R[i0 + i] = ld2(i0 + i);
    SCHED();
  };
  if (w < 9) {
    const int lane = fresh_lane(), tid = w * 64 + lane;
    int flo, fhi;
    bool alo, ahi;
    fin_map(tid, flo, fhi, alo, ahi);
    const u32x2 xa = *(LAS u32x2*)(L + L_X + (4 * alo + (lane & 3)) * 1152 + flo * 2), xb = *(LAS u32x2*)(L + L_X + (4 * ahi + (lane & 3)) * 1152 + fhi * 2);
    const f32x4 gal = *(LAS f32x4*)(L + L_MV + flo * 4), gah = *(LAS f32x4*)(L + L_MV + fhi * 4);
    const int w3 = w - 3 * ((w * 11) >> 5);
    const float ia = w3 == 0 ? 1.f / 80 : (w3 == 1 ? 1.f / 48 : 1.f / 64), ib = w3 == 0 ? 1.f / 48 : (w3 == 1 ? 1.f / 80 : 1.f / 64);
    BAR();
    STAMP(12);
    u32x4 yp[4];
#pragma unroll
    for (int p = 0; p < 4; ++p) yp[p] = cld128(rWs, tid * 16, YP_OFF + ((tb32 * 4 + q) * 4 + p) * 9216);
    SCHED();
    window(0);
    const f32x4 sbl = *(LAS f32x4*)(L + L_MV + 2304 + flo * 4), sbh = *(LAS f32x4*)(L + L_MV + 2304 + fhi * 4);
    const f32x4 cbl = *(LAS f32x4*)(L + L_MV + 4608 + flo * 4), cbh = *(LAS f32x4*)(L + L_MV + 4608 + fhi * 4);
    f32x4 yl = (up4(yp[0][0], yp[0][1]) + up4(yp[1][0], yp[1][1])) + (up4(yp[2][0], yp[2][1]) + up4(yp[3][0], yp[3][1]));
    f32x4 yh = (up4(yp[0][2], yp[0][3]) + up4(yp[1][2], yp[1][3])) + (up4(yp[2][2], yp[2][3]) + up4(yp[3][2], yp[3][3]));
    const u32x4 x2 = pk8(up4(xa.x, xa.y) + gal * yl, up4(xb.x, xb.y) + gah * yh);
    STAMP(22);
    yl = up4(x2[0], x2[1]);
    yh = up4(x2[2], x2[3]);
    {
      const float lo = (yl[0] + yl[1]) + (yl[2] + yl[3]), hi = (yh[0] + yh[1]) + (yh[2] + yh[3]);
      const float ma = sum16((alo ? 0.f : lo) + (ahi ? 0.f : hi)) * ia, mb = sum16((alo ? lo : 0.f) + (ahi ? hi : 0.f)) * ib;
      const f32x4 dl = yl - (alo ? mb : ma), dh = yh - (ahi ? mb : ma), ql = dl * dl, qh = dh * dh;
      const float q0 = (ql[0] + ql[1]) + (ql[2] + ql[3]), q1 = (qh[0] + qh[1]) + (qh[2] + qh[3]);
      const float qa = sum16((alo ? 0.f : q0) + (ahi ? 0.f : q1)), qb = sum16((alo ? q0 : 0.f) + (ahi ? q1 : 0.f));
      if (lane < 4) *(LAS f32x4*)(L + L_ST + (w * 4 + lane) * 16) = f32x4{ma, mb, qa, qb};
    }
    BAR();
    *(LAS u32x4*)(L + L_X + tid * 16) = x2;
    f32x4 t[9];
#pragma unroll
    for (int k = 0; k < 9; ++k) t[k] = *(LAS f32x4*)(L + L_ST + (k * 4 + (lane & 3)) * 16);
    float ma = 0.f, mb = 0.f, va = 0.f, vb = 0.f;
#pragma unroll
    for (int k = 0; k < 9; ++k) {
      ma += t[k][0] * (k % 3 == 0 ? 80.f / D : (k % 3 == 1 ? 48.f / D : 64.f / D));
      mb += t[k][1] * (k % 3 == 0 ? 48.f / D : (k % 3 == 1 ? 80.f / D : 64.f / D));
    }
#pragma unroll
    for (int k = 0; k < 9; ++k) {
      const float da = t[k][0] - ma, db = t[k][1] - mb;
      va += t[k][2] + da * da * (k % 3 == 0 ? 80.f : (k % 3 == 1 ? 48.f : 64.f));
      vb += t[k][3] + db * db * (k % 3 == 0 ? 48.f : (k % 3 == 1 ? 80.f : 64.f));
    }
    va = rsqrtf(va * (1.f / D) + 1e-5f);
    vb = rsqrtf(vb * (1.f / D) + 1e-5f);
    *(LAS u32x2*)(L + L_HS + (flo >> 5) * 512 + alo * 256 + (lane & 15) * 16 + (flo & 4) * 2) = pk4((yl - (alo ? mb : ma)) * (alo ? vb : va) * cbl + sbl);
    *(LAS u32x2*)(L + L_HS + (fhi >> 5) * 512 + ahi * 256 + (lane & 15) * 16 + (fhi & 4) * 2) = pk4((yh - (ahi ? mb : ma)) * (ahi ? vb : va) * cbh + sbh);
    BAR();
    STAMP(13);
    BAR();
    window(9);
    BAR();
    gather_h2();
  } else if (w == 9) {
    const int lane = fresh_lane();
    if (lane == 0) fst(ws + F3_OFF + (tb32 * 4 + q) * 128, tag);
    if (lane == 1 && job) fst(ws + F6_OFF + b * 8, tag);
    poll(lane < 32 ? ws + F3_OFF + (tb32 * 4 + (lane & 3)) * 128 : ws + F6_OFF + (27 * sl + (lane < 59 ? lane - 32 : 0)) * 8, tag);
    BAR();
    STAMP(12);
    BAR();
    BAR();
    STAMP(13);
    u32x4 h[9];
#pragma unroll
    for (int i = 0; i < 9; ++i) h[i] = *(LAS u32x4*)(L + L_HS + i * 1024 + vl16);
#pragma unroll
    for (int i = 0; i < 9; ++i) cst128(h[i], rWs, vl16, H2_OFF + (rt * 8 + sl) * 9216 + i * 1024);
    VMW(0);
    if (lane == 0) fst(ws + F4_OFF + (rt * 8 + sl) * 128, tag);
    STAMP(23);
    BAR();
    window(0);
    window(9);
    poll(ws + F4_OFF + (rt * 8 + (lane & 7)) * 128, tag);
    BAR();
    gather_h2();
  } else {
    BAR();
    STAMP(12);
    window(0);
    BAR();
    BAR();
    STAMP(13);
    BAR();
    window(9);
    BAR();
    gather_h2();
  }
  const int fp = (3 * w + 1) >> 1, fh = (3 * (w & ~1) + 2) >> 1;
#pragma unroll
  for (int ph = 0; ph < 2; ++ph) {
    LAS char* A = L + ph * L_H1 + vl16;
    u32x4 Bf[2][4];
#pragma unroll
    for (int mb = 0; mb < 4; ++mb) Bf[0][mb] = *(LAS u32x4*)(A + mb * 1024);
#pragma unroll
    for (int kb = 0; kb < 18; ++kb) {
      if (kb < 17) {
#pragma unroll
        for (int mb = 0; mb < 4; ++mb) Bf[(kb + 1) & 1][mb] = *(LAS u32x4*)(A + ((kb + 1) * 4 + mb) * 1024);
      }
      SCHED();
#pragma unroll
      for (int j = 0; j < 3; ++j) {
        const int it = ph * 54 + kb * 3 + j;
        if (it >= 18) asm volatile("s_waitcnt vmcnt(%1)" : "+v"(R[it % 18]) : "i"(it < 90 ? 17 : 107 - it));
#pragma unroll
        for (int mb = 0; mb < 4; ++mb) mac(acc[mb][j], R[it % 18], Bf[kb & 1][mb], kb == 0);
        SCHED();
        if (it + 18 < 108)
          refill(R[it % 18], vl16, sWs, (it + 18 < 54 ? b1 : b2 - 54 * 1024) + (it + 18) * 1024);
        SCHED();
      }
    }
    MFMA_DONE();
    STAMP(16 + 2 * ph);
    if (ph == 0) {
      LAS char* H = L + L_H1 + vl16;
#pragma unroll
      for (int mb = 0; mb < 4; ++mb) {
        *(LAS u32x4*)(H + (fp * 4 + mb) * 1024) = pk8(relu4(acc[mb][0]), relu4(acc[mb][1]));
        *(LAS u32x2*)(H + (fh * 4 + mb) * 1024 + (w & 1) * 8) = pk4(relu4(acc[mb][2]));
      }
      BAR();
      STAMP(17);
    }
  }
  {
    const int lane = fresh_lane(), vp2 = ((lane >> 2) & 3) * 73728 + (lane >> 4) * 64 + (lane & 3) * 16;
#pragma unroll
    for (int h = 0; h < 2; ++h) {
      const int ob = OP_OFF + ((rt * 8 + 4 * h) * 8 + sl) * 9216 + w * 768;
      cst128(pk8(acc[2 * h][0], acc[2 * h][1]), rWs, vp2, ob);
      cst128(pk8(acc[2 * h + 1][0], acc[2 * h + 1][1]), rWs, vp2, ob + 256);
      cst128(pk8(acc[2 * h][2], acc[2 * h + 1][2]), rWs, vp2, ob + 512);
    }
  }
  VMW(0);
  BAR();
  STAMP(19);
  if (w < 9) {
    const int lane = fresh_lane(), tid = w * 64 + lane;
    int flo, fhi;
    bool alo, ahi;
    fin_map(tid, flo, fhi, alo, ahi);
    const u32x4 x2 = *(LAS u32x4*)(L + L_X + tid * 16);
    const f32x4 gl = *(LAS f32x4*)(L + L_GB + flo * 4), gh = *(LAS f32x4*)(L + L_GB + fhi * 4);
    const rsrc_t rO = mkrs(out, S * D * 2);
    const int olo = (16 * alo + (lane & 3)) * 1152 + flo * 2, ohi = (16 * ahi + (lane & 3)) * 1152 + fhi * 2;
    BAR();
    STAMP(20);
    u32x4 p[8];
#pragma unroll
    for (int k = 0; k < 8; ++k) p[k] = cld128(rWs, tid * 16, OP_OFF + ((rt * 8 + sl) * 8 + k) * 9216);
    f32x4 sl4 = {0.f, 0.f, 0.f, 0.f}, sh4 = {0.f, 0.f, 0.f, 0.f};
#pragma unroll
    for (int k = 0; k < 8; ++k) {
      sl4 += up4(p[k][0], p[k][1]);
      sh4 += up4(p[k][2], p[k][3]);
    }
    __builtin_amdgcn_raw_buffer_store_b64(pk4(up4(x2[0], x2[1]) + gl * sl4), rO, olo, (32 * tb32 + 4 * q) * 1152, 0);
    __builtin_amdgcn_raw_buffer_store_b64(pk4(up4(x2[2], x2[3]) + gh * sh4), rO, ohi, (32 * tb32 + 4 * q) * 1152, 0);
  } else {
    if (w == 9) {
      const int lane = fresh_lane();
      if (lane == 0) fst(ws + F5_OFF + (rt * 8 + sl) * 128, tag);
      poll(ws + F5_OFF + (rt * 8 + (lane & 7)) * 128, tag);
    }
    BAR();
    STAMP(20);
  }
  STAMP(21);
}

extern "C" void kernel_launch(void* const* d_in, const int* in_sizes, int n_in, void* d_out, int out_size, void* d_ws, size_t ws_size,
                              hipStream_t stream) {
  (void)hipFuncSetAttribute((const void*)fused_block, hipFuncAttributeMaxDynamicSharedMemorySize, LDS_BYTES);
  fused_block<<<256, 768, LDS_BYTES, stream>>>((const u16*)d_in[0], (const u16*)d_in[1], (const u16*)d_in[2], (const u16*)d_in[3],
                                               (const u16*)d_in[4], (const u16*)d_in[6], (const u16*)d_in[7], (const u16*)d_in[8],
                                               (const u16*)d_in[9], (char*)d_ws, (u16*)d_out);
}
```

```cpp
#include <hip/hip_runtime.h>
#include <hip/hip_bf16.h>
#include <stdint.h>
#include <utility>

typedef unsigned short u16;
typedef unsigned long long u64;
typedef __attribute__((ext_vector_type(2))) float f32x2;
typedef __attribute__((ext_vector_type(4))) float f32x4;
typedef __attribute__((ext_vector_type(2))) __bf16 bf16x2;
typedef __attribute__((ext_vector_type(4))) short s16x4;
typedef __attribute__((ext_vector_type(2))) unsigned u32x2;
typedef __attribute__((ext_vector_type(3))) unsigned u32x3;
typedef __attribute__((ext_vector_type(4))) unsigned u32x4;
typedef __amdgpu_buffer_rsrc_t rsrc_t;
#define LAS __attribute__((address_space(3)))
typedef __attribute__((address_space(1))) u64 gu64;

constexpr int S = 2048, D = 576, F = 4608;
#define WDP 0
#define ROT 1
#define CLR 7
#define EXP2 0
#define XP 1
#define CROT 1
#define C0NT 0
#define TCH 1
#define T0 1
constexpr unsigned E_OFF = 0, G0_OFF = 0x1000, G1_OFF = 0x2000, GW_OFF = 0x3000, G3_OFF = 0x5000;
constexpr unsigned MG_OFF = 0x8000;
constexpr unsigned MV_OFF = 0x2c000;
constexpr unsigned AB_OFF = 0x30000;
constexpr unsigned XB_OFF = AB_OFF + 256 * 18432;
constexpr unsigned WT_OFF = XB_OFF + 256 * 46080;
constexpr unsigned OP_OFF = WT_OFF + 96 * 110592;
constexpr unsigned WS_END = OP_OFF + 2048 * 9216;
constexpr int SC1 = 16;
constexpr int OOB = (int)0x80000000u;
constexpr int L_H = 65536, L_SA = 139264, L_MP = 143872, L_PR = 144384, L_RED = 0;
constexpr int L_H1 = 73728, L_ST = 137216, L_MV = 140544, L_GB = 147456, L_X2 = 149760, L_CT = 158976, L_STAMP = 159232;
constexpr int C_MP = L_CT + 16, C_PR = L_CT + 20, C_MV = L_CT + 28;
constexpr int LDS_BYTES = 163840;
constexpr int SPIN_CAP = 1 << 16;

template <int N>
using ic = std::integral_constant<int, N>;
template <class Fn, int... I>
__device__ __forceinline__ void sfor_(Fn&& f, std::integer_sequence<int, I...>) { (f(std::integral_constant<int, I>{}), ...); }
template <int N, class Fn>
__device__ __forceinline__ void sfor(Fn&& f) { sfor_(f, std::make_integer_sequence<int, N>{}); }

__device__ __forceinline__ rsrc_t mkrs(const void* p, unsigned bytes) { return __builtin_amdgcn_make_buffer_rsrc((void*)p, 0, bytes, 0x00020000); }
__device__ __forceinline__ u32x4 mksrd(const void* p, unsigned bytes) {
  const u64 a = (u64)p;
  return u32x4{(unsigned)a, (unsigned)(a >> 32) & 0xffffu, bytes, 0x00020000u};
}
#define SCHED() __builtin_amdgcn_sched_barrier(0)
__device__ __forceinline__ void cst128(u32x4 d, rsrc_t r, int v, int s) {
  __builtin_amdgcn_raw_buffer_store_b128(d, r, v, s, SC1);
  SCHED();
  asm volatile("s_nop 1" ::: "memory");
  SCHED();
}
template <bool NT>
__device__ __forceinline__ void dma(unsigned lds, int voff, u32x4 srd, int soff) {
  if (NT) asm volatile("s_mov_b32 m0, %0\n\ts_nop 3\n\tbuffer_load_dwordx4 %1, %2, %3 offen nt lds" ::"s"(lds), "v"(voff), "s"(srd), "s"(soff) : "memory");
  else asm volatile("s_mov_b32 m0, %0\n\ts_nop 3\n\tbuffer_load_dwordx4 %1, %2, %3 offen lds" ::"s"(lds), "v"(voff), "s"(srd), "s"(soff) : "memory");
}
template <int P>
__device__ __forceinline__ void fill(u32x4& r, int voff, u32x4 srd, int soff) {
  if (P == 1) asm volatile("buffer_load_dwordx4 %0, %1, %2, %3 offen sc1" : "=v"(r) : "v"(voff), "s"(srd), "s"(soff) : "memory");
  else if (P == 2) asm volatile("buffer_load_dwordx4 %0, %1, %2, %3 offen nt" : "=v"(r) : "v"(voff), "s"(srd), "s"(soff) : "memory");
  else asm volatile("buffer_load_dwordx4 %0, %1, %2, %3 offen" : "=v"(r) : "v"(voff), "s"(srd), "s"(soff) : "memory");
}
__device__ __forceinline__ void refill(u32x4& r, int voff, u32x4 srd, int soff) {
  asm volatile("buffer_load_dwordx4 %0, %1, %2, %3 offen sc1" : "+v"(r) : "v"(voff), "s"(srd), "s"(soff) : "memory");
}
template <int P>
__device__ __forceinline__ void fill64(u32x2& r, int voff, u32x4 srd, int soff) {
  if (P == 1) asm volatile("buffer_load_dwordx2 %0, %1, %2, %3 offen sc1" : "=v"(r) : "v"(voff), "s"(srd), "s"(soff) : "memory");
  else asm volatile("buffer_load_dwordx2 %0, %1, %2, %3 offen" : "=v"(r) : "v"(voff), "s"(srd), "s"(soff) : "memory");
}
#define BAR() asm volatile("s_waitcnt lgkmcnt(0)\n\ts_barrier" ::: "memory")
#define VMW(n) asm volatile("s_waitcnt vmcnt(%0)" ::"i"(n) : "memory")
#define VMW_SEL(n, d, lim, ...) \
  asm volatile("s_waitcnt vmcnt(%[hi])\n\ts_cmp_lt_u32 %[ww], %[li]\n\ts_cbranch_scc1 1f\n\ts_waitcnt vmcnt(%[lo])\n1:" \
               : __VA_ARGS__ : [lo] "i"(n), [hi] "i"((n) + (d)), [li] "i"(lim), [ww] "s"(w) : "scc", "memory")
#define PIN(x) asm volatile("" : "+v"(x))
__device__ __forceinline__ int fresh_lane() {
  int l;
  asm volatile("v_mbcnt_lo_u32_b32 %0, -1, 0\n\tv_mbcnt_hi_u32_b32 %0, -1, %0" : "=v"(l));
  return l;
}
__device__ __forceinline__ float bflo(unsigned u) { return __uint_as_float(u << 16); }
__device__ __forceinline__ float bfhi(unsigned u) { return __uint_as_float(u & 0xffff0000u); }
__device__ __forceinline__ unsigned pk(float a, float b) {
  f32x2 v = {a, b};
  return __builtin_bit_cast(unsigned, __builtin_convertvector(v, bf16x2));
}
__device__ __forceinline__ float rbf(float a) { return bflo(pk(a, 0.f)); }
__device__ __forceinline__ u32x2 pk4(f32x4 a) { return u32x2{pk(a[0], a[1]), pk(a[2], a[3])}; }
__device__ __forceinline__ u32x4 pk8(f32x4 a, f32x4 b) { return u32x4{pk(a[0], a[1]), pk(a[2], a[3]), pk(b[0], b[1]), pk(b[2], b[3])}; }
__device__ __forceinline__ f32x4 up4(unsigned a, unsigned b) { return f32x4{bflo(a), bfhi(a), bflo(b), bfhi(b)}; }
__device__ __forceinline__ f32x4 relu4(f32x4 a) { return f32x4{fmaxf(a[0], 0.f), fmaxf(a[1], 0.f), fmaxf(a[2], 0.f), fmaxf(a[3], 0.f)}; }
__device__ __forceinline__ u32x4 live(u32x4 v) {
  return u32x4{min(v.x, 0xfffffffeu), min(v.y, 0xfffffffeu), min(v.z, 0xfffffffeu), min(v.w, 0xfffffffeu)};
}
__device__ __forceinline__ u32x4 blank() {
  u32x4 o;
  asm volatile("v_mov_b32 %0, -1" : "=v"(o.x));
  asm volatile("v_mov_b32 %0, -1" : "=v"(o.y));
  asm volatile("v_mov_b32 %0, -1" : "=v"(o.z));
  asm volatile("v_mov_b32 %0, -1" : "=v"(o.w));
  return o;
}
__device__ __forceinline__ unsigned top(unsigned m, u32x4 v) { return max(max(m, max(v.x, v.y)), max(v.z, v.w)); }
__device__ __forceinline__ bool absent(unsigned m) { return __builtin_amdgcn_ballot_w64(m == 0xffffffffu) != 0; }
__device__ __forceinline__ void mac(f32x4& c, u32x4 a, u32x4 b, bool first) {
  if (first) asm("v_mfma_f32_16x16x32_bf16 %0, %1, %2, 0" : "=&v"(c) : "v"(a), "v"(b));
  else asm("v_mfma_f32_16x16x32_bf16 %0, %1, %2, %0" : "+v"(c) : "v"(a), "v"(b));
}
#define MFMA_DONE() do { SCHED(); asm volatile("s_nop 7"); SCHED(); } while (0)
template <int C>
__device__ __forceinline__ float dpp(float v) { return __uint_as_float(__builtin_amdgcn_update_dpp(0, __float_as_uint(v), C, 0xf, 0xf, false)); }
template <int C>
__device__ __forceinline__ float dppadd(float v) { return v + dpp<C>(v); }
__device__ __forceinline__ float swap16add(float a, float b) {
  auto r = __builtin_amdgcn_permlane16_swap(__float_as_uint(a), __float_as_uint(b), false, false);
  return __uint_as_float(r[0]) + __uint_as_float(r[1]);
}
__device__ __forceinline__ float swap32add(float a, float b) {
  auto r = __builtin_amdgcn_permlane32_swap(__float_as_uint(a), __float_as_uint(b), false, false);
  return __uint_as_float(r[0]) + __uint_as_float(r[1]);
}
__device__ __forceinline__ float sum16(float v) {
  v = dppadd<0x128>(dppadd<0x124>(v));
  v = swap16add(v, v);
  return swap32add(v, v);
}
__device__ __forceinline__ float colsums(f32x4 a, f32x4 b, bool hi8) {
  const float t0 = swap32add(a[0], b[0]), t1 = swap32add(a[1], b[1]), t2 = swap32add(a[2], b[2]), t3 = swap32add(a[3], b[3]);
  const float u0 = swap16add(t0, t2), u1 = swap16add(t1, t3);
  return (hi8 ? u1 : u0) + dpp<0x128>(hi8 ? u0 : u1);
}
__device__ __forceinline__ float rowsums(const float (&s)[8], float e, bool hi8) {
  const float z = colsums(f32x4{s[0], s[1], s[2], s[3]}, f32x4{s[4], s[5], s[6], s[7]}, hi8) + e;
  return dppadd<0x141>(dppadd<0x4e>(dppadd<0xb1>(z)));
}
__device__ __forceinline__ void fst(char* p, u64 v) { __hip_atomic_store((gu64*)p, v, __ATOMIC_RELAXED, __HIP_MEMORY_SCOPE_AGENT); }
__device__ __forceinline__ void lds_wait(LAS char* p, unsigned v) {
  _Pragma("unroll 1") for (int it = 0; it < SPIN_CAP * 16; ++it) {
    if (__hip_atomic_load((LAS unsigned*)p, __ATOMIC_RELAXED, __HIP_MEMORY_SCOPE_WORKGROUP) == v) break;
    __builtin_amdgcn_s_sleep(1);
  }
  asm volatile("" ::: "memory");
}
__device__ __forceinline__ unsigned lds_add(LAS char* p) {
  asm volatile("s_waitcnt lgkmcnt(0)" ::: "memory");
  return __hip_atomic_fetch_add((LAS unsigned*)p, 1u, __ATOMIC_RELAXED, __HIP_MEMORY_SCOPE_WORKGROUP);
}
__device__ __forceinline__ void owner(int f, int& w, int& j) {
  w = (f * 43) >> 7;
  const int r = f - 3 * w;
  j = (w & 1) ? (r == 0 ? 2 : r - 1) : r;
}
constexpr int due(int j) { return (j * 101) / 89; }
constexpr int issued(int it) {
  int n = 0;
  for (int j = 0; j < 90; ++j) n += due(j) < it;
  return n;
}
constexpr int X_W2 = 5, X_OP = 80;
constexpr int extra(int it) { return (it < 36 && it % 6 == 2) + (it == X_W2) + (it == X_OP); }
constexpr int younger(int it) {
  int n = issued(it) - (it - 18) - 1;
  for (int s = due(it - 18); s < it; ++s) n += extra(s);
  return n;
}

#define STAMP(i)

__global__ __launch_bounds__(768, 3) void fused_block(const u16* __restrict__ x, const u16* __restrict__ wre, const u16* __restrict__ wim,
                                                      const u16* __restrict__ dre, const u16* __restrict__ dim, const u16* __restrict__ w1,
                                                      const u16* __restrict__ w2, const u16* __restrict__ cvec, const u16* __restrict__ wmod,
                                                      char* __restrict__ ws, u16* __restrict__ out) {
  extern __shared__ __attribute__((aligned(16))) char smem[];
  LAS char* L = (LAS char*)smem;
  const unsigned lb = (unsigned)(size_t)L;
  const int w = __builtin_amdgcn_readfirstlane(threadIdx.x >> 6);
  const int b = blockIdx.x, x8 = b & 7, idx = b >> 3, g = idx & 3, j8 = idx >> 2;
  const int mat = x8 >> 2, rta = 8 * (x8 & 3) + j8;
  const int kq4 = x8 >> 1, kh = kq4 & 1, R = 8 * (x8 & 1) + j8;
  const int rt = 2 * R + (g >> 1), sl = 2 * kq4 + (g & 1), pc = rt * 8 + sl;
  const int si = rt & 15;
  const int kq = w & 3, cg = w >> 2, crot = CROT == 1 ? g : (CROT == 2 ? j8 : (CROT == 3 ? j8 + (g >> 1) * 2 : 0));
  const bool job = idx < 27;
  const rsrc_t rWs = mkrs(ws, WS_END);
  const u32x4 sWs = mksrd(ws, WS_END), sS = mksrd(mat ? wim : wre, S * S * 2);
  const int abown = AB_OFF + ((mat * 4 + g) * 32 + rta) * 18432;
  if (threadIdx.x == 0) *(LAS u32x4*)(L + C_MP) = u32x4{0u, 0u, 0u, 0u};
  BAR();
  STAMP(0);
  auto ws_dma = [&](auto NT, int c, int n, int vdma) {
    dma<NT>(lb + (c & 1) * 65536 + n * 1024, vdma, sS, (128 * R + 8 * (n & 15)) * 4096 + kh * 2048 + ((c + crot) & 3) * 512 + (n >> 4) * 128);
  };
  auto mk_vdma = [&]() {
    const int lane = fresh_lane();
    return (lane >> 3) * 4096 + (((lane & 7) ^ ((4 * (w & 1) + (lane >> 4)) & 7)) * 16);
  };
  u64 tag;
  auto get_tag = [&]() {
    const u32x2 t = *(LAS u32x2*)(L + L_CT);
    tag = ((u64)(unsigned)__builtin_amdgcn_readfirstlane(t.y) << 32) | (unsigned)__builtin_amdgcn_readfirstlane(t.x);
  };
  auto await = [&](u32x2& v, int voff) {
    int n = SPIN_CAP;
    asm volatile(
        "1:\n\tv_cmp_ne_u64 vcc, %[tg], %[v]\n\ts_cbranch_vccz 2f\n\ts_sleep 2\n\tbuffer_load_dwordx2 %[v], %[vo], %[rs], 0 offen sc1\n\t"
        "s_sub_u32 %[n], %[n], 1\n\ts_waitcnt vmcnt(0)\n\ts_cbranch_scc0 1b\n2:"
        : [v] "+v"(v), [n] "+s"(n)
        : [tg] "s"(tag), [vo] "v"(voff), [rs] "s"(sWs)
        : "vcc", "scc", "memory");
  };
  u32x4 V[9], Wd[18], cw;
  u32x2 ev;
  unsigned cx;
  const bool mjob = b < 36;
  const u32x4 sC = mksrd(cvec, D * 2), sD = mksrd(mat ? dim : dre, D * D * 2);
  const int wr = (w + 2 * g) & 7;
  unsigned wm;
  __builtin_amdgcn_s_setprio(2);
  if (w >= 4) {
    const int lane = fresh_lane();
    const u32x4 sX = mksrd(x, S * D * 2);
    const int so = (64 * rta + 8 * wr) * 1152;
#pragma unroll
    for (int r = 0; r < 8; ++r) fill<XP>(V[r], lane * 16, sX, so + r * 1152);
    fill<XP>(V[8], (lane >> 3) * 1152 + 1024 + (lane & 7) * 16, sX, so);
  } else {
    const int lane = fresh_lane(), rb = 288 * (b & 1) + 72 * w;
    fill64<1>(ev, 0, sWs, E_OFF);
    asm volatile("buffer_load_dword %0, %1, %2, %3 offen sc1" : "=v"(wm) : "v"(TCH && idx >= 5 && w ? lane * 128 : OOB), "s"(sD), "s"(((idx - 5) * 3 + w - 1) * 8192) : "memory");
    if (mjob) {
      const u32x4 sM = mksrd(wmod, D * 6 * D * 2);
      const int so = rb * 6912 + (b >> 1) * 128;
#pragma unroll
      for (int i = 0; i < 8; ++i) fill<2>(V[i], (lane >> 3) * 8 * 6912 + (lane & 7) * 16, sM, so + i * 6912);
      fill<2>(V[8], (lane >> 3) * 6912 + (lane & 7) * 16, sM, so + 64 * 6912);
      fill<0>(cw, (lane >> 3) * 16, sC, rb * 2);
      asm volatile("buffer_load_ushort %0, %1, %2, %3 offen" : "=v"(cx) : "v"((lane >> 3) * 2), "s"(sC), "s"(rb * 2 + 128) : "memory");
    }
  }
  __builtin_amdgcn_s_setprio(0);
  STAMP(6);
  auto wd_dma = [&](int q0, auto NQ) {
    constexpr int nq = NQ;
    const int lane = fresh_lane(), f = 9 * g + (w < 8 ? w : 8);
    const int vo = (8 * (lane >> 5) + ((lane >> 3) & 3)) * 1152 + (((lane & 7) ^ (lane >> 4)) * 16);
    const int so = (32 * (f >> 1) + 4 * (f & 1)) * 1152 + q0 * 128;
#pragma unroll
    for (int i = 0; i < nq; ++i) {
      dma<WDP == 2>(lb + w * 10240 + i * 2048, vo, sD, so + i * 128);
      dma<WDP == 2>(lb + w * 10240 + i * 2048 + 1024, vo ^ 64, sD, so + i * 128 + 16 * 1152);
    }
  };
  auto wd_get = [&](auto K0, auto N) {
    constexpr int k0 = K0, n = N;
    const int lane = fresh_lane();
    const int p = w * 10240 + (lane & 15) * 128 + (((lane >> 4) ^ ((lane >> 1) & 7)) * 16);
    const int pe = w >= 10 ? p ^ 64 : p, po = w >= 10 ? p + 2048 : p ^ 64;
#pragma unroll
    for (int i = 0; i < n; ++i) Wd[k0 + i] = *(LAS u32x4*)(L + ((i & 1) ? po : pe) + (i >> 1) * 2048);
#pragma unroll
    for (int i = 0; i < n; ++i) PIN(Wd[k0 + i]);
  };
  wd_dma(w >= 10 ? 4 : 0, ic<5>{});
  STAMP(7);
  asm volatile("s_waitcnt vmcnt(10)"
               : "+v"(V[0]), "+v"(V[1]), "+v"(V[2]), "+v"(V[3]), "+v"(V[4]), "+v"(V[5]), "+v"(V[6]), "+v"(V[7]), "+v"(V[8]), "+v"(ev), "+v"(cw),
                 "+v"(cx), "+v"(wm));
  STAMP(1);
  auto silu = [&](float c) { return rbf(c / (1.f + __expf(-c))); };
  if (w >= 4) {
    const int lane = fresh_lane();
    float s1[8], s2[8], e1, e2, pv = 0.f;
    auto sums = [&](u32x4 v, float p, float& a, float& q) {
      const f32x4 d0 = up4(v.x, v.y) - p, d1 = up4(v.z, v.w) - p, t = d0 + d1, u = d0 * d0 + d1 * d1;
      a = (t[0] + t[1]) + (t[2] + t[3]);
      q = (u[0] + u[1]) + (u[2] + u[3]);
    };
#pragma unroll
    for (int r = 0; r < 8; ++r) {
      const unsigned v0 = V[r].x;
      const float p = bflo(__builtin_amdgcn_readfirstlane(v0));
      sums(V[r], p, s1[r], s2[r]);
      pv = (lane >> 3) == r ? p : pv;
      PIN(V[r]);
      if (r & 1) SCHED();
    }
    sums(V[8], pv, e1, e2);
    PIN(V[8]);
    const float m = rowsums(s1, e1, lane & 8) * (1.f / D), var = rowsums(s2, e2, lane & 8) * (1.f / D) - m * m;
    float rs = rsqrtf(fmaxf(var, 0.f) + 1e-5f), sh = -(pv + m) * rs;
    PIN(rs);
    PIN(sh);
    STAMP(2);
    VMW(0);
    wd_get(ic<0>{}, ic<10>{});
    if (w < 8) {
      wd_dma(5, ic<4>{});
    } else {
      const u32x4 ones = blank();
      const int vl = (CLR & 4) ? fresh_lane() * 16 : OOB;
#pragma unroll
      for (int i = 0; i < 5; ++i) cst128(ones, rWs, w - 8 + 4 * i < 18 ? vl : OOB, abown + (w - 8 + 4 * i) * 1024);
    }
    STAMP(29);
#pragma unroll
    for (int r = 0; r < 9; ++r) {
      const float a = r < 8 ? __uint_as_float(__builtin_amdgcn_readlane(__float_as_uint(rs), 8 * (r & 7))) : rs;
      const float c = r < 8 ? __uint_as_float(__builtin_amdgcn_readlane(__float_as_uint(sh), 8 * (r & 7))) : sh;
      V[r] = pk8(up4(V[r].x, V[r].y) * a + c, up4(V[r].z, V[r].w) * a + c);
      PIN(V[r]);
    }
    if (w < 8) VMW(0);
  } else {
    const int lane = fresh_lane();
    tag = (((u64)(unsigned)__builtin_amdgcn_readfirstlane(ev.y) << 32) | (unsigned)__builtin_amdgcn_readfirstlane(ev.x)) + 1;
    const unsigned tl = (unsigned)tag;
    if (w == 0 && lane == 0) *(LAS u64*)(L + L_CT) = tag;
    bool last = false;
    f32x2 v;
    if (mjob) {
      f32x4 a0 = {0.f, 0.f, 0.f, 0.f}, a1 = a0;
#pragma unroll
      for (int i = 0; i < 9; ++i) {
        const unsigned u = i < 8 ? cw[i >> 1] : cx;
        const float sv = silu((i & 1) ? bfhi(u) : bflo(u));
        a0 += sv * up4(V[i].x, V[i].y);
        a1 += sv * up4(V[i].z, V[i].w);
      }
      *(LAS float*)(L + L_MP + w * 256 + (lane & 7) * 32 + (lane >> 3) * 4) = colsums(a0, a1, lane & 8);
      unsigned n = 0;
      if (lane == 0) n = lds_add(L + C_MP);
      last = __builtin_amdgcn_readfirstlane(n) == 3;
      asm volatile("" ::: "memory");
      LAS char* p = L + L_MP + (lane & 31) * 8;
      v = (*(LAS f32x2*)p + *(LAS f32x2*)(p + 256)) + (*(LAS f32x2*)(p + 512) + *(LAS f32x2*)(p + 768));
    }
#pragma unroll
    for (int i = 0; i < 4; ++i)
      cst128(u32x4{__float_as_uint(v.x), tl, __float_as_uint(v.y), tl}, rWs, last ? (lane >> 5) * 18432 + (lane & 31) * 16 : OOB,
             MG_OFF + i * 36864 + (b & 1) * 9216 + (b >> 1) * 512);
    STAMP(2);
    VMW(4);
    wd_get(ic<0>{}, ic<10>{});
    wd_dma(5, ic<4>{});
    STAMP(29);
    const int mo = MG_OFF + x8 * 18432 + w * 1024, vl = lane * 16, v2 = w == 0 ? vl : OOB;
    u32x4 p[6];
    _Pragma("unroll 1") for (int it = 0; it < SPIN_CAP; ++it) {
#pragma unroll
      for (int i = 0; i < 6; ++i) fill<1>(p[i], i < 4 ? vl : v2, sWs, mo + (i >> 1) * 4096 + (i & 1) * 9216);
      asm volatile("s_waitcnt vmcnt(0)" : "+v"(p[0]), "+v"(p[1]), "+v"(p[2]), "+v"(p[3]), "+v"(p[4]), "+v"(p[5]));
      unsigned d = 0, e = 0;
#pragma unroll
      for (int i = 0; i < 4; ++i) d |= (p[i].y ^ tl) | (p[i].w ^ tl);
#pragma unroll
      for (int i = 4; i < 6; ++i) e |= (p[i].y ^ tl) | (p[i].w ^ tl);
      if (__builtin_amdgcn_ballot_w64((w == 0 ? d | e : d) != 0) == 0) break;
      __builtin_amdgcn_s_sleep(2);
    }
#pragma unroll
    for (int i = 0; i < 3; ++i) {
      unsigned o = pk(__uint_as_float(p[2 * i].x) + __uint_as_float(p[2 * i + 1].x), __uint_as_float(p[2 * i].z) + __uint_as_float(p[2 * i + 1].z));
      if ((w + 4 * i) * 64 + lane >= 288) o = pk(bflo(o) + 1.f, bfhi(o) + 1.f);
      if (i < 2 || w == 0) *(LAS f32x2*)(L + L_SA + (w + 4 * i) * 512 + lane * 8) = f32x2{bflo(o), bfhi(o)};
    }
  }
  STAMP(30);
  u32x4 Wr[6];
  u32x3 cv3;
  const int bn = idx * 12 + w, bph = bn >= 162, bkb = ((bn - 162 * bph) * 57) >> 9, bnb = bn - 162 * bph - 9 * bkb, jm = x8 * 5 + idx - 27;
  {
    const int lane = fresh_lane();
    const int pitch = job ? (bph ? 1152 : F * 2) : 6912, step = job ? 8 * pitch : pitch;
    const int so = job ? (32 * bkb + (bph ? x8 * 576 : 0)) * pitch + (bph ? 0 : x8 * 1152) + bnb * 128 : 48 * w * 6912 + 2304 + jm * 128;
    const u32x4 sW = mksrd(job ? (bph ? w2 : w1) : wmod, job ? D * F * 2 : (jm < 36 ? D * 6 * D * 2 : 0u));
    const int vo = (lane >> 3) * (job ? pitch : 6 * 6912) + (lane & 7) * 16;
#pragma unroll
    for (int i = 0; i < 6; ++i) fill<2>(Wr[i], i < 4 || !job ? vo : OOB, sW, so + i * step);
    asm volatile("buffer_load_dwordx3 %0, %1, %2, %3 offen" : "=v"(cv3) : "v"((lane >> 3) * 12), "s"(sC), "s"(96 * w) : "memory");
  }
  STAMP(31);
  if (w < 8) wd_get(ic<10>{}, ic<8>{});
  BAR();
  STAMP(3);
  u32x2 gv;
  if (w >= 4) {
    get_tag();
    const int lane = fresh_lane();
    LAS char* sp = L + L_SA + lane * 32;
    LAS char* sq = L + L_SA + 2048 + (lane & 7) * 32;
    const f32x4 sa0 = *(LAS f32x4*)sp, sa1 = *(LAS f32x4*)(sp + 16), ca0 = *(LAS f32x4*)(sp + 2304), ca1 = *(LAS f32x4*)(sp + 2320);
    const int hb = L_H + (wr >> 2) * 2048 + (lane & 3) * 256 + (((4 * wr) ^ (lane & 7)) & 15) * 16;
#pragma unroll
    for (int r = 0; r < 8; ++r) {
      *(LAS u32x4*)(L + ((hb + (lane >> 2) * 4096) ^ ((r & 3) * 16)) + (r >> 2) * 1024) = pk8(up4(V[r].x, V[r].y) * ca0 + sa0, up4(V[r].z, V[r].w) * ca1 + sa1);
      if (r & 1) SCHED();
    }
    const f32x4 ta0 = *(LAS f32x4*)sq, ta1 = *(LAS f32x4*)(sq + 16), da0 = *(LAS f32x4*)(sq + 2304), da1 = *(LAS f32x4*)(sq + 2320);
    *(LAS u32x4*)(L + ((hb + 65536 + (lane & 4) * 1024 + (lane >> 5) * 1024) ^ (((lane >> 3) & 3) * 16))) =
        pk8(up4(V[8].x, V[8].y) * da0 + ta0, up4(V[8].z, V[8].w) * da1 + ta1);
    VMW(7);
  } else {
    const int vdma = mk_vdma();
#pragma unroll
    for (int i = 0; i < 16; ++i) ws_dma(std::integral_constant<bool, C0NT>{}, 0, w + 4 * i, vdma);
  }
  BAR();
  STAMP(4);
  {
    const int lane = fresh_lane();
    const int ra = L_H + (lane >> 4) * 256 + (((lane & 15) ^ (lane >> 4)) * 16);
    const unsigned neg = mat ? 0x80008000u : 0u;
    int fw, fj;
    owner(9 * g + (w < 8 ? w : 8), fw, fj);
    const int so = abown + (fw - 3 * g) * 6144 + fj * 1024;
    f32x4 c[4];
    if (w < 8) {
      if (w == 4 && lane == 0) fst(ws + G0_OFF + ((mat * 4 + g) * 32 + rta) * 8, tag);
      u32x4 Hf[2][4];
#pragma unroll
      for (int rb = 0; rb < 4; ++rb) Hf[0][rb] = *(LAS u32x4*)(L + ra + rb * 1024);
      sfor<18>([&](auto KB) {
        constexpr int kb = KB;
        if (kb < 17) {
#pragma unroll
          for (int rb = 0; rb < 4; ++rb) Hf[(kb + 1) & 1][rb] = *(LAS u32x4*)(L + (ra ^ ((kb + 1) & 1) * 64) + (kb + 1) * 4096 + rb * 1024);
        }
        SCHED();
#pragma unroll
        for (int rb = 0; rb < 4; ++rb) mac(c[rb], Hf[kb & 1][rb], Wd[kb], kb == 0);
        SCHED();
      });
      MFMA_DONE();
      cst128(live(pk8(c[0], c[1]) ^ neg), rWs, lane * 16, so);
      cst128(live(pk8(c[2], c[3]) ^ neg), rWs, lane * 16, so + 3072);
      VMW_SEL(2, 16, 4, );
    } else {
      const int g0 = G0_OFF + ((mat * 4 + g) * 32 + 16 * kh + (lane & 15)) * 8;
      fill64<1>(gv, w == 11 ? g0 : OOB, sWs, 0);
      const int rk = (ra ^ (w >= 10 ? 64 : 0)) + (w >= 10 ? 9 * 4096 : 0) + (w & 1) * 2048;
      u32x4 Hf[4][2];
      auto ldh = [&](int i) {
        Hf[i & 3][0] = *(LAS u32x4*)(L + (rk ^ (i & 1) * 64) + i * 4096);
        Hf[i & 3][1] = *(LAS u32x4*)(L + (rk ^ (i & 1) * 64) + i * 4096 + 1024);
      };
      ldh(0);
      ldh(1);
      ldh(2);
#pragma unroll
      for (int i = 0; i < 9; ++i) {
        if (i < 6) ldh(i + 3);
        SCHED();
        mac(c[0], Hf[i & 3][0], Wd[i], i == 0);
        mac(c[1], Hf[i & 3][1], Wd[i], i == 0);
        SCHED();
      }
      MFMA_DONE();
      LAS char* pp = L + L_PR + (w & 1) * 2048 + lane * 16;
      if (w >= 10) {
        *(LAS f32x4*)pp = c[0];
        *(LAS f32x4*)(pp + 1024) = c[1];
        if (lane == 0) lds_add(L + C_PR + (w & 1) * 4);
        asm volatile("s_waitcnt vmcnt(0)" : "+v"(gv));
        if (w == 11) await(gv, g0);
      } else {
        lds_wait(L + C_PR + (w & 1) * 4, 1);
        cst128(live(pk8(c[0] + *(LAS f32x4*)pp, c[1] + *(LAS f32x4*)(pp + 1024)) ^ neg), rWs, lane * 16, so + (w & 1) * 3072);
        asm volatile("s_waitcnt vmcnt(1)" : "+v"(gv));
      }
    }
  }
#pragma unroll
  for (int i = 0; i < 6; ++i) PIN(Wr[i]);
  PIN(cv3);
  STAMP(5);
  BAR();
  int vl16, rd0;
  {
    const int lane = fresh_lane();
    vl16 = lane * 16;
    rd0 = kq * 16384 + (lane & 15) * 128 + (((lane >> 4) ^ ((lane & 15) >> 1)) * 16);
    LAS char* sp = L + L_H + w * 4096;
    if (job) {
#pragma unroll
      for (int i = 0; i < 4; ++i) *(LAS u32x4*)(sp + i * 1024 + vl16) = Wr[i];
      LAS char* rp = sp + ((lane >> 4) * 8 + ((lane >> 2) & 3)) * 128 + (lane & 3) * 16;
#pragma unroll
      for (int i = 0; i < 4; ++i) {
        const u32x2 lo = __builtin_bit_cast(u32x2, __builtin_amdgcn_ds_read_tr16_b64_v4i16((LAS s16x4*)(rp + (i >> 1) * 64 + (i & 1) * 8)));
        const u32x2 hi = __builtin_bit_cast(u32x2, __builtin_amdgcn_ds_read_tr16_b64_v4i16((LAS s16x4*)(rp + (i >> 1) * 64 + (i & 1) * 8 + 512)));
        Wr[i] = u32x4{lo.x, lo.y, hi.x, hi.y};
      }
    } else {
      f32x4 a0 = {0.f, 0.f, 0.f, 0.f}, a1 = a0;
#pragma unroll
      for (int i = 0; i < 6; ++i) {
        const unsigned u = cv3[i >> 1];
        const float sv = silu((i & 1) ? bfhi(u) : bflo(u));
        a0 += sv * up4(Wr[i].x, Wr[i].y);
        a1 += sv * up4(Wr[i].z, Wr[i].w);
      }
      *(LAS float*)(sp + (lane & 7) * 32 + (lane >> 3) * 4) = colsums(a0, a1, lane & 8);
      unsigned n = 0;
      if (lane == 0) n = lds_add(L + C_MV);
      const bool last = __builtin_amdgcn_readfirstlane(n) == 11;
      asm volatile("" ::: "memory");
      f32x4 v = {0.f, 0.f, 0.f, 0.f};
#pragma unroll
      for (int k = 0; k < 12; ++k) v += *(LAS f32x4*)(L + L_H + k * 4096 + (lane & 15) * 16);
      u32x2 o = pk4(v);
      if (jm >= 18 && jm < 27) o = pk4(up4(o.x, o.y) + 1.f);
      cst128(u32x4{o.x, (unsigned)tag, o.y, (unsigned)tag}, rWs, last && jm < 36 && lane < 16 ? vl16 : OOB, MV_OFF + jm * 256);
    }
  }
  const int abin = AB_OFF + ((mat * 4 + g) * 32 + 16 * kh + kq) * 18432 + cg * 6144;
  auto ab_at = [&](int c) { return abin + ((c + crot) & 3) * 73728; };
  u32x4 Ra[6];
  _Pragma("unroll 1") for (int it = 0; it < SPIN_CAP; ++it) {
#pragma unroll
    for (int i = 0; i < 6; ++i) fill<1>(Ra[i], vl16, sWs, ab_at(0) + i * 1024);
    asm volatile("s_waitcnt vmcnt(0)" : "+v"(Ra[0]), "+v"(Ra[1]), "+v"(Ra[2]), "+v"(Ra[3]), "+v"(Ra[4]), "+v"(Ra[5]));
    unsigned m = 0;
#pragma unroll
    for (int i = 0; i < 6; ++i) m = top(m, Ra[i]);
    if (!absent(m)) break;
  }
  STAMP(8);
#pragma unroll
  for (int i = 0; i < 4; ++i) {
    int fw, fj;
    owner(4 * bnb + i, fw, fj);
    cst128(Wr[i], rWs, job ? vl16 : OOB, WT_OFF + (x8 * 12 + fw) * 110592 + (bph * 54 + bkb * 3 + fj) * 1024);
  }
  BAR();
  STAMP(9);
  f32x4 acc[8][3];
  auto touch = [&](unsigned& r, int ph) {
    const int lane = fresh_lane();
    asm volatile("buffer_load_dword %0, %1, %2, %3 offen sc1"
                 : "=v"(r)
                 : "v"(lane < 27 && (T0 || ph) ? lane * 128 : OOB), "s"(sWs), "s"(WT_OFF + (sl * 12 + w) * 110592 + ph * 55296 + si * 3456)
                 : "memory");
  };
  {
    u32x4 Wf[2][4];
    auto ldw = [&](int s, int h) {
#pragma unroll
      for (int r = 0; r < 4; ++r) Wf[h][r] = *(LAS u32x4*)(L + ((s >> 1) & 1) * 65536 + (4 * h + r) * 2048 + (rd0 ^ ((s & 1) * 64)));
    };
    sfor<8>([&](auto SS) {
      constexpr int s = SS, c = s >> 1, kk = s & 1;
      if constexpr (s >= 2) {
        constexpr int y = s == 7 ? 1 : (s == 6 ? 3 : (kk ? 9 : 4));
        asm volatile("s_waitcnt vmcnt(%3)" : "+v"(Ra[3 * kk]), "+v"(Ra[3 * kk + 1]), "+v"(Ra[3 * kk + 2]) : "i"(y));
      }
      if constexpr (kk == 0) {
        if constexpr (s > 0) BAR();
        STAMP(10 + c);
        ldw(s, 0);
        SCHED();
        if constexpr (c < 3) {
          const int vdma = mk_vdma();
#pragma unroll
          for (int i = 0; i < 5; ++i) ws_dma(std::false_type{}, c + 1, w + 12 * i, vdma);
          if (w < 4) ws_dma(std::false_type{}, c + 1, 60 + w, vdma);
        }
        if constexpr (s == 2) {
          if (w == 11 && fresh_lane() == 0) fst(ws + GW_OFF + (x8 * 32 + idx) * 8, tag);
        }
        if constexpr (s == 4) fill64<1>(gv, GW_OFF + (sl * 32 + (fresh_lane() & 31)) * 8, sWs, 0);
        if constexpr (s == 6) {
          if (w == 11 && fresh_lane() == 0) fst(ws + G1_OFF + pc * 8, tag);
          await(gv, GW_OFF + (sl * 32 + (fresh_lane() & 31)) * 8);
          touch(wm, 0);
        }
      }
      if constexpr (s < 4) cst128(blank(), rWs, (CLR & 1) && (s < 3 || w < 9) ? vl16 : OOB, XB_OFF + pc * 46080 + (w + 12 * s) * 1024);
      if constexpr (s >= 2) {
        if (absent(top(top(top(0u, Ra[3 * kk]), Ra[3 * kk + 1]), Ra[3 * kk + 2]))) {
          _Pragma("unroll 1") for (int it = 0; it < SPIN_CAP; ++it) {
#pragma unroll
            for (int j = 0; j < 3; ++j) refill(Ra[3 * kk + j], vl16, sWs, ab_at(c) + kk * 3072 + j * 1024);
            asm volatile("s_waitcnt vmcnt(0)" : "+v"(Ra[3 * kk]), "+v"(Ra[3 * kk + 1]), "+v"(Ra[3 * kk + 2]));
            if (!absent(top(top(top(0u, Ra[3 * kk]), Ra[3 * kk + 1]), Ra[3 * kk + 2]))) break;
          }
        }
      }
      SCHED();
      ldw(s, 1);
      SCHED();
#pragma unroll
      for (int j = 0; j < 3; ++j)
#pragma unroll
        for (int r = 0; r < 4; ++r) mac(acc[r][j], Ra[3 * kk + j], Wf[0][r], s == 0);
      SCHED();
      if constexpr (kk == 0) ldw(s + 1, 0);
      SCHED();
#pragma unroll
      for (int j = 0; j < 3; ++j) {
#pragma unroll
        for (int r = 0; r < 4; ++r) mac(acc[4 + r][j], Ra[3 * kk + j], Wf[1][r], s == 0);
        SCHED();
        if constexpr (c < 3) refill(Ra[3 * kk + j], vl16, sWs, ab_at(c + 1) + kk * 3072 + j * 1024);
        SCHED();
      }
    });
  }
  MFMA_DONE();
  STAMP(14);
  auto fin_map = [&](int lane, int& flo, int& fhi, bool& alo, bool& ahi) {
    const int s = lane >> 4, f2 = 3 * w + ((w & 1) ? 0 : 2);
    flo = 8 * ((lane >> 2) & 3) + (s < 2 ? 32 * ((3 * w + 1) >> 1) : 32 * (f2 >> 1) + 4 * (f2 & 1));
    fhi = flo + (s < 2 ? 4 : 0);
    alo = s == 1;
    ahi = s >= 1;
  };
  const int row0 = 32 * (pc >> 2) + 4 * (pc & 3);
  u32x2 xa, xb;
  u32x4 mv;
  int gvo;
  {
    const int lane = fresh_lane();
    int flo, fhi;
    bool alo, ahi;
    fin_map(lane, flo, fhi, alo, ahi);
    const u32x4 sX = mksrd(x, S * D * 2);
    gvo = G1_OFF + (16 * R + (lane & 15)) * 8;
    fill64<1>(gv, gvo, sWs, 0);
    fill64<0>(xa, lane < 48 ? (16 * alo + (lane & 3)) * 1152 + flo * 2 : OOB, sX, row0 * 1152);
    fill64<0>(xb, lane < 48 ? (16 * ahi + (lane & 3)) * 1152 + fhi * 2 : OOB, sX, row0 * 1152);
    fill<1>(mv, w < 9 ? vl16 : OOB, sWs, MV_OFF + w * 1024);
  }
  BAR();
  f32x4 own[2][3];
#pragma unroll
  for (int d = 0; d < 4; ++d) {
    if (d != kq) {
      LAS char* p = L + L_RED + ((cg * 4 + d) * 3 + ((kq - d - 1) & 3)) * 3072 + vl16;
      *(LAS u32x4*)p = pk8(acc[2 * d][0], acc[2 * d][1]);
      *(LAS u32x4*)(p + 1024) = pk8(acc[2 * d + 1][0], acc[2 * d + 1][1]);
      *(LAS u32x4*)(p + 2048) = pk8(acc[2 * d][2], acc[2 * d + 1][2]);
    } else {
#pragma unroll
      for (int j = 0; j < 3; ++j) {
        own[0][j] = acc[2 * d][j];
        own[1][j] = acc[2 * d + 1][j];
      }
    }
  }
  BAR();
  STAMP(15);
  {
    LAS char* p = L + L_RED + w * 9216 + vl16;
#pragma unroll
    for (int s = 0; s < 3; ++s) {
      const u32x4 a = *(LAS u32x4*)(p + s * 3072), c = *(LAS u32x4*)(p + s * 3072 + 1024), e = *(LAS u32x4*)(p + s * 3072 + 2048);
      own[0][0] += up4(a.x, a.y);
      own[0][1] += up4(a.z, a.w);
      own[1][0] += up4(c.x, c.y);
      own[1][1] += up4(c.z, c.w);
      own[0][2] += up4(e.x, e.y);
      own[1][2] += up4(e.z, e.w);
    }
    asm volatile("s_waitcnt vmcnt(3)" : "+v"(gv), "+v"(wm));
    await(gv, gvo);
    const int lane = fresh_lane(), vpub = ((lane >> 2) & 3) * 46080 + (lane >> 4) * 64 + (lane & 3) * 16;
    const int yb = XB_OFF + (4 * R + kq) * 4 * 46080 + kq4 * 9216 + (3 * g + cg) * 768;
    cst128(live(pk8(own[0][0], own[0][1])), rWs, vpub, yb);
    cst128(live(pk8(own[1][0], own[1][1])), rWs, vpub, yb + 256);
    cst128(live(pk8(own[0][2], own[1][2])), rWs, vpub, yb + 512);
  }
  STAMP(16);
  asm volatile("s_waitcnt vmcnt(3)" : "+v"(xa), "+v"(xb), "+v"(mv));
  if (w < 9) {
    _Pragma("unroll 1") for (int it = 0; it < SPIN_CAP; ++it) {
      if (__builtin_amdgcn_ballot_w64(((mv.y ^ (unsigned)tag) | (mv.w ^ (unsigned)tag)) != 0) == 0) break;
      refill(mv, vl16, sWs, MV_OFF + w * 1024);
      asm volatile("s_waitcnt vmcnt(0)" : "+v"(mv));
    }
    *(LAS f32x4*)(L + L_MV + w * 1024 + vl16) = up4(mv.x, mv.z);
  }
  const int rot = ROT ? (si * 18) >> 4 : 0, wb = WT_OFF + (sl * 12 + w) * 110592;
  auto w_at = [&](int it) {
    const int ph = it / 54, k = (it - 54 * ph) / 3 + rot;
    return wb + ph * 55296 + (k >= 18 ? k - 18 : k) * 3072 + (it % 3) * 1024;
  };
  u32x4 Rw[18];
#pragma unroll
  for (int i = 0; i < 9; ++i) fill<1>(Rw[i], vl16, sWs, w_at(i));
  BAR();
  STAMP(17);
  {
    const int lane = fresh_lane(), v48 = lane < 48 ? vl16 : OOB;
    int flo, fhi;
    bool alo, ahi;
    fin_map(lane, flo, fhi, alo, ahi);
    u32x4 yp[4];
    _Pragma("unroll 1") for (int it = 0; it < SPIN_CAP; ++it) {
#pragma unroll
      for (int p = 0; p < 4; ++p) fill<1>(yp[p], v48, sWs, XB_OFF + pc * 46080 + p * 9216 + w * 768);
      asm volatile("s_waitcnt vmcnt(0)" : "+v"(yp[0]), "+v"(yp[1]), "+v"(yp[2]), "+v"(yp[3]));
      if (!absent(top(top(top(top(0u, yp[0]), yp[1]), yp[2]), yp[3]))) break;
      __builtin_amdgcn_s_sleep(2);
    }
    STAMP(18);
    const f32x4 gal = *(LAS f32x4*)(L + L_MV + flo * 4), gah = *(LAS f32x4*)(L + L_MV + fhi * 4);
    const f32x4 sbl = *(LAS f32x4*)(L + L_MV + 2304 + flo * 4), sbh = *(LAS f32x4*)(L + L_MV + 2304 + fhi * 4);
    const f32x4 cbl = *(LAS f32x4*)(L + L_MV + 4608 + flo * 4), cbh = *(LAS f32x4*)(L + L_MV + 4608 + fhi * 4);
    f32x4 yl = (up4(yp[0].x, yp[0].y) + up4(yp[1].x, yp[1].y)) + (up4(yp[2].x, yp[2].y) + up4(yp[3].x, yp[3].y));
    f32x4 yh = (up4(yp[0].z, yp[0].w) + up4(yp[1].z, yp[1].w)) + (up4(yp[2].z, yp[2].w) + up4(yp[3].z, yp[3].w));
    const u32x4 x2 = pk8(up4(xa.x, xa.y) + gal * yl, up4(xb.x, xb.y) + gah * yh);
    yl = up4(x2.x, x2.y);
    yh = up4(x2.z, x2.w);
    {
      const bool on = lane < 48;
      const float lo = (yl[0] + yl[1]) + (yl[2] + yl[3]), hi = (yh[0] + yh[1]) + (yh[2] + yh[3]);
      const float ma = sum16((alo ? 0.f : lo) + (ahi ? 0.f : hi)) * (1.f / 48), mb = sum16((alo ? lo : 0.f) + (ahi ? hi : 0.f)) * (1.f / 48);
      const f32x4 dl = yl - (alo ? mb : ma), dh = yh - (ahi ? mb : ma), ql = dl * dl, qh = dh * dh;
      const float q0 = on ? (ql[0] + ql[1]) + (ql[2] + ql[3]) : 0.f, q1 = on ? (qh[0] + qh[1]) + (qh[2] + qh[3]) : 0.f;
      const float qa = sum16((alo ? 0.f : q0) + (ahi ? 0.f : q1)), qb = sum16((alo ? q0 : 0.f) + (ahi ? q1 : 0.f));
      if (lane < 4) *(LAS f32x4*)(L + L_ST + (w * 4 + lane) * 16) = f32x4{ma, mb, qa, qb};
    }
    BAR();
    STAMP(19);
    f32x4 t[12];
#pragma unroll
    for (int k = 0; k < 12; ++k) t[k] = *(LAS f32x4*)(L + L_ST + (k * 4 + (lane & 3)) * 16);
    float ma = 0.f, mb = 0.f, va = 0.f, vb = 0.f, da = 0.f, db = 0.f;
#pragma unroll
    for (int k = 0; k < 12; ++k) {
      ma += t[k][0];
      mb += t[k][1];
      va += t[k][2];
      vb += t[k][3];
    }
    ma *= 1.f / 12;
    mb *= 1.f / 12;
#pragma unroll
    for (int k = 0; k < 12; ++k) {
      da += (t[k][0] - ma) * (t[k][0] - ma);
      db += (t[k][1] - mb) * (t[k][1] - mb);
    }
    va = rsqrtf((va + 48.f * da) * (1.f / D) + 1e-5f);
    vb = rsqrtf((vb + 48.f * db) * (1.f / D) + 1e-5f);
    cst128(live(pk8((yl - (alo ? mb : ma)) * (alo ? vb : va) * cbl + sbl, (yh - (ahi ? mb : ma)) * (ahi ? vb : va) * cbh + sbh)), rWs, v48,
           XB_OFF + pc * 46080 + 36864 + w * 768);
    if (w == 11 && b == 0 && lane == 0) fst(ws + E_OFF, tag);
#pragma unroll
    for (int i = 9; i < 18; ++i) fill<1>(Rw[i], vl16, sWs, w_at(i));
    if (lane < 48) *(LAS u32x4*)(L + L_X2 + w * 768 + vl16) = x2;
    STAMP(20);
    const int kp = (flo >> 5) - rot, lg = (lane >> 2) & 3;
    const int hl = (kp < 0 ? kp + 18 : kp) * 4096 + lg * 256 + (lg & 1) * 192 + (lane & 3) * 16 + (flo & 4) * 2;
    const int olo = hl + alo * 1024, ohi = hl + ahi * 1024 + (fhi - flo) * 2;
    u32x4 h[8];
    _Pragma("unroll 1") for (int it = 0; it < SPIN_CAP; ++it) {
#pragma unroll
      for (int p = 0; p < 8; ++p) fill<1>(h[p], v48, sWs, XB_OFF + (rt * 8 + p) * 46080 + 36864 + w * 768);
      asm volatile("s_waitcnt vmcnt(0)" : "+v"(h[0]), "+v"(h[1]), "+v"(h[2]), "+v"(h[3]), "+v"(h[4]), "+v"(h[5]), "+v"(h[6]), "+v"(h[7]));
      unsigned m = 0;
#pragma unroll
      for (int p = 0; p < 8; ++p) m = top(m, h[p]);
      if (!absent(m)) break;
      __builtin_amdgcn_s_sleep(2);
    }
    STAMP(21);
    if (lane < 48) {
#pragma unroll
      for (int p = 0; p < 8; ++p) {
        *(LAS u32x2*)(L + (olo ^ ((p & 3) * 64)) + (p >> 2) * 2048) = u32x2{h[p].x, h[p].y};
        *(LAS u32x2*)(L + (ohi ^ ((p & 3) * 64)) + (p >> 2) * 2048) = u32x2{h[p].z, h[p].w};
      }
    }
  }
#pragma unroll
  for (int i = 0; i < 18; ++i) PIN(Rw[i]);
  BAR();
  STAMP(22);
  f32x4 am[4][3];
  {
    sfor<2>([&](auto PH) {
      constexpr int ph = PH;
      LAS char* A = L + ph * L_H1 + (vl16 ^ ((fresh_lane() & 16) * 12));
      u32x4 Bf[2][4];
#pragma unroll
      for (int mb = 0; mb < 4; ++mb) Bf[0][mb] = *(LAS u32x4*)(A + mb * 1024);
      sfor<18>([&](auto KB) {
        constexpr int kb = KB;
        if (kb < 17) {
#pragma unroll
          for (int mb = 0; mb < 4; ++mb) Bf[(kb + 1) & 1][mb] = *(LAS u32x4*)(A + ((kb + 1) * 4 + mb) * 1024);
        }
        SCHED();
        sfor<3>([&](auto J) {
          constexpr int j = J, it = ph * 54 + kb * 3 + j;
          if constexpr (it >= 18) asm volatile("s_waitcnt vmcnt(%1)" : "+v"(Rw[it % 18]) : "i"(younger(it)));
#pragma unroll
          for (int mb = 0; mb < 4; ++mb) mac(am[mb][j], Rw[it % 18], Bf[kb & 1][mb], kb == 0);
          SCHED();
          constexpr int j0 = issued(it), j1 = issued(it + 1);
          sfor<j1 - j0>([&](auto JJ) {
            constexpr int jj = j0 + JJ;
            static_assert(jj <= it, "slot still live");
            refill(Rw[jj % 18], vl16, sWs, w_at(jj + 18));
          });
          if constexpr (it < 36 && it % 6 == 2) cst128(blank(), rWs, (CLR & 2) ? vl16 : OOB, OP_OFF + pc * 73728 + (w + 12 * (it / 6)) * 1024);
          if constexpr (it == X_W2) touch(wm, 1);
          if constexpr (it == 53) PIN(wm);
          if constexpr (it == X_OP) fill64<1>(gv, G3_OFF + (rt * 8 + (fresh_lane() & 7)) * 8, sWs, 0);
          SCHED();
        });
      });
      MFMA_DONE();
      STAMP(23 + 2 * ph);
      if constexpr (ph == 0) {
        const int fp = ((3 * w + 1) >> 1) - rot, fh = ((3 * (w & ~1) + 2) >> 1) - rot;
        LAS char* Hp = A + L_H1 + (fp < 0 ? fp + 18 : fp) * 4096;
        LAS char* Hh = A + L_H1 + (fh < 0 ? fh + 18 : fh) * 4096 + (w & 1) * 8;
#pragma unroll
        for (int mb = 0; mb < 4; ++mb) {
          *(LAS u32x4*)(Hp + mb * 1024) = pk8(relu4(am[mb][0]), relu4(am[mb][1]));
          *(LAS u32x2*)(Hh + mb * 1024) = pk4(relu4(am[mb][2]));
        }
        BAR();
        if (w == 0 && fresh_lane() == 0) fst(ws + G3_OFF + pc * 8, tag);
        STAMP(24);
      }
    });
  }
  {
    const int lane = fresh_lane(), v48 = lane < 48 ? vl16 : OOB;
    await(gv, G3_OFF + (rt * 8 + (lane & 7)) * 8);
    const int vp2 = ((lane >> 2) & 3) * 73728 + (lane >> 4) * 64 + (lane & 3) * 16;
#pragma unroll
    for (int h = 0; h < 2; ++h) {
      const int ob = OP_OFF + ((rt * 8 + 4 * h) * 8 + sl) * 9216 + w * 768;
      cst128(live(pk8(am[2 * h][0], am[2 * h][1])), rWs, vp2, ob);
      cst128(live(pk8(am[2 * h + 1][0], am[2 * h + 1][1])), rWs, vp2, ob + 256);
      cst128(live(pk8(am[2 * h][2], am[2 * h + 1][2])), rWs, vp2, ob + 512);
    }
    STAMP(26);
    BAR();
    int flo, fhi;
    bool alo, ahi;
    fin_map(lane, flo, fhi, alo, ahi);
    const u32x4 x2 = *(LAS u32x4*)(L + L_X2 + w * 768 + vl16);
    const f32x4 gl = *(LAS f32x4*)(L + L_GB + flo * 4), gh = *(LAS f32x4*)(L + L_GB + fhi * 4);
    u32x4 p[8];
    _Pragma("unroll 1") for (int it = 0; it < SPIN_CAP; ++it) {
#pragma unroll
      for (int k = 0; k < 8; ++k) fill<1>(p[k], v48, sWs, OP_OFF + (pc * 8 + k) * 9216 + w * 768);
      asm volatile("s_waitcnt vmcnt(0)" : "+v"(p[0]), "+v"(p[1]), "+v"(p[2]), "+v"(p[3]), "+v"(p[4]), "+v"(p[5]), "+v"(p[6]), "+v"(p[7]));
      unsigned m = 0;
#pragma unroll
      for (int k = 0; k < 8; ++k) m = top(m, p[k]);
      if (!absent(m)) break;
      __builtin_amdgcn_s_sleep(2);
    }
    STAMP(27);
    f32x4 sl4 = {0.f, 0.f, 0.f, 0.f}, sh4 = {0.f, 0.f, 0.f, 0.f};
#pragma unroll
    for (int k = 0; k < 8; ++k) {
      sl4 += up4(p[k].x, p[k].y);
      sh4 += up4(p[k].z, p[k].w);
    }
    const rsrc_t rO = mkrs(out, S * D * 2);
    const int olo = lane < 48 ? (16 * alo + (lane & 3)) * 1152 + flo * 2 : OOB, ohi = lane < 48 ? (16 * ahi + (lane & 3)) * 1152 + fhi * 2 : OOB;
    __builtin_amdgcn_raw_buffer_store_b64(pk4(up4(x2.x, x2.y) + gl * sl4), rO, olo, row0 * 1152, 0);
    __builtin_amdgcn_raw_buffer_store_b64(pk4(up4(x2.z, x2.w) + gh * sh4), rO, ohi, row0 * 1152, 0);
  }
  STAMP(28);
}

extern "C" void kernel_launch(void* const* d_in, const int* in_sizes, int n_in, void* d_out, int out_size, void* d_ws, size_t ws_size,
                              hipStream_t stream) {
  (void)hipFuncSetAttribute((const void*)fused_block, hipFuncAttributeMaxDynamicSharedMemorySize, LDS_BYTES);
  fused_block<<<256, 768, LDS_BYTES, stream>>>((const u16*)d_in[0], (const u16*)d_in[1], (const u16*)d_in[2], (const u16*)d_in[3],
                                               (const u16*)d_in[4], (const u16*)d_in[6], (const u16*)d_in[7], (const u16*)d_in[8],
                                               (const u16*)d_in[9], (char*)d_ws, (u16*)d_out);
}
```

```cpp
#define STAMPS
#define PRT
#include <stdio.h>
#include <hip/hip_runtime.h>
#include <hip/hip_bf16.h>
#include <stdint.h>
#include <utility>

typedef unsigned short u16;
typedef unsigned long long u64;
typedef __attribute__((ext_vector_type(2))) float f32x2;
typedef __attribute__((ext_vector_type(4))) float f32x4;
typedef __attribute__((ext_vector_type(2))) __bf16 bf16x2;
typedef __attribute__((ext_vector_type(4))) short s16x4;
typedef __attribute__((ext_vector_type(2))) unsigned u32x2;
typedef __attribute__((ext_vector_type(3))) unsigned u32x3;
typedef __attribute__((ext_vector_type(4))) unsigned u32x4;
typedef __amdgpu_buffer_rsrc_t rsrc_t;
#define LAS __attribute__((address_space(3)))
typedef __attribute__((address_space(1))) u64 gu64;

constexpr int S = 2048, D = 576, F = 4608;
#define WDP 0
#define ROT 1
#define CLR 7
#define EXP2 0
#define XP 1
#define CROT 1
#define C0NT 0
#define TCH 1
#define T0 1
constexpr unsigned E_OFF = 0, G0_OFF = 0x1000, G1_OFF = 0x2000, GW_OFF = 0x3000, G3_OFF = 0x5000;
constexpr unsigned MG_OFF = 0x8000;
constexpr unsigned MV_OFF = 0x2c000;
constexpr unsigned AB_OFF = 0x30000;
constexpr unsigned XB_OFF = AB_OFF + 256 * 18432;
constexpr unsigned WT_OFF = XB_OFF + 256 * 46080;
constexpr unsigned OP_OFF = WT_OFF + 96 * 110592;
constexpr unsigned WS_END = OP_OFF + 2048 * 9216;
constexpr int SC1 = 16;
constexpr int OOB = (int)0x80000000u;
constexpr int L_H = 65536, L_SA = 139264, L_MP = 143872, L_PR = 144384, L_RED = 0;
constexpr int L_H1 = 73728, L_ST = 137216, L_MV = 140544, L_GB = 147456, L_X2 = 149760, L_CT = 158976, L_STAMP = 159232;
constexpr int C_MP = L_CT + 16, C_PR = L_CT + 20, C_MV = L_CT + 28;
constexpr int LDS_BYTES = 163840;
constexpr int SPIN_CAP = 1 << 16;

template <int N>
using ic = std::integral_constant<int, N>;
template <class Fn, int... I>
__device__ __forceinline__ void sfor_(Fn&& f, std::integer_sequence<int, I...>) { (f(std::integral_constant<int, I>{}), ...); }
template <int N, class Fn>
__device__ __forceinline__ void sfor(Fn&& f) { sfor_(f, std::make_integer_sequence<int, N>{}); }

__device__ __forceinline__ rsrc_t mkrs(const void* p, unsigned bytes) { return __builtin_amdgcn_make_buffer_rsrc((void*)p, 0, bytes, 0x00020000); }
__device__ __forceinline__ u32x4 mksrd(const void* p, unsigned bytes) {
  const u64 a = (u64)p;
  return u32x4{(unsigned)a, (unsigned)(a >> 32) & 0xffffu, bytes, 0x00020000u};
}
#define SCHED() __builtin_amdgcn_sched_barrier(0)
__device__ __forceinline__ void cst128(u32x4 d, rsrc_t r, int v, int s) {
  __builtin_amdgcn_raw_buffer_store_b128(d, r, v, s, SC1);
  SCHED();
  asm volatile("s_nop 1" ::: "memory");
  SCHED();
}
template <bool NT>
__device__ __forceinline__ void dma(unsigned lds, int voff, u32x4 srd, int soff) {
  if (NT) asm volatile("s_mov_b32 m0, %0\n\ts_nop 3\n\tbuffer_load_dwordx4 %1, %2, %3 offen nt lds" ::"s"(lds), "v"(voff), "s"(srd), "s"(soff) : "memory");
  else asm volatile("s_mov_b32 m0, %0\n\ts_nop 3\n\tbuffer_load_dwordx4 %1, %2, %3 offen lds" ::"s"(lds), "v"(voff), "s"(srd), "s"(soff) : "memory");
}
template <int P>
__device__ __forceinline__ void fill(u32x4& r, int voff, u32x4 srd, int soff) {
  if (P == 1) asm volatile("buffer_load_dwordx4 %0, %1, %2, %3 offen sc1" : "=v"(r) : "v"(voff), "s"(srd), "s"(soff) : "memory");
  else if (P == 2) asm volatile("buffer_load_dwordx4 %0, %1, %2, %3 offen nt" : "=v"(r) : "v"(voff), "s"(srd), "s"(soff) : "memory");
  else asm volatile("buffer_load_dwordx4 %0, %1, %2, %3 offen" : "=v"(r) : "v"(voff), "s"(srd), "s"(soff) : "memory");
}
__device__ __forceinline__ void refill(u32x4& r, int voff, u32x4 srd, int soff) {
  asm volatile("buffer_load_dwordx4 %0, %1, %2, %3 offen sc1" : "+v"(r) : "v"(voff), "s"(srd), "s"(soff) : "memory");
}
template <int P>
__device__ __forceinline__ void fill64(u32x2& r, int voff, u32x4 srd, int soff) {
  if (P == 1) asm volatile("buffer_load_dwordx2 %0, %1, %2, %3 offen sc1" : "=v"(r) : "v"(voff), "s"(srd), "s"(soff) : "memory");
  else asm volatile("buffer_load_dwordx2 %0, %1, %2, %3 offen" : "=v"(r) : "v"(voff), "s"(srd), "s"(soff) : "memory");
}
#define BAR() asm volatile("s_waitcnt lgkmcnt(0)\n\ts_barrier" ::: "memory")
#define VMW(n) asm volatile("s_waitcnt vmcnt(%0)" ::"i"(n) : "memory")
#define VMW_SEL(n, d, lim, ...) \
  asm volatile("s_waitcnt vmcnt(%[hi])\n\ts_cmp_lt_u32 %[ww], %[li]\n\ts_cbranch_scc1 1f\n\ts_waitcnt vmcnt(%[lo])\n1:" \
               : __VA_ARGS__ : [lo] "i"(n), [hi] "i"((n) + (d)), [li] "i"(lim), [ww] "s"(w) : "scc", "memory")
#define PIN(x) asm volatile("" : "+v"(x))
__device__ __forceinline__ int fresh_lane() {
  int l;
  asm volatile("v_mbcnt_lo_u32_b32 %0, -1, 0\n\tv_mbcnt_hi_u32_b32 %0, -1, %0" : "=v"(l));
  return l;
}
__device__ __forceinline__ float bflo(unsigned u) { return __uint_as_float(u << 16); }
__device__ __forceinline__ float bfhi(unsigned u) { return __uint_as_float(u & 0xffff0000u); }
__device__ __forceinline__ unsigned pk(float a, float b) {
  f32x2 v = {a, b};
  return __builtin_bit_cast(unsigned, __builtin_convertvector(v, bf16x2));
}
__device__ __forceinline__ float rbf(float a) { return bflo(pk(a, 0.f)); }
__device__ __forceinline__ u32x2 pk4(f32x4 a) { return u32x2{pk(a[0], a[1]), pk(a[2], a[3])}; }
__device__ __forceinline__ u32x4 pk8(f32x4 a, f32x4 b) { return u32x4{pk(a[0], a[1]), pk(a[2], a[3]), pk(b[0], b[1]), pk(b[2], b[3])}; }
__device__ __forceinline__ f32x4 up4(unsigned a, unsigned b) { return f32x4{bflo(a), bfhi(a), bflo(b), bfhi(b)}; }
__device__ __forceinline__ f32x4 relu4(f32x4 a) { return f32x4{fmaxf(a[0], 0.f), fmaxf(a[1], 0.f), fmaxf(a[2], 0.f), fmaxf(a[3], 0.f)}; }
__device__ __forceinline__ u32x4 live(u32x4 v) {
  return u32x4{min(v.x, 0xfffffffeu), min(v.y, 0xfffffffeu), min(v.z, 0xfffffffeu), min(v.w, 0xfffffffeu)};
}
__device__ __forceinline__ u32x4 blank() {
  u32x4 o;
  asm volatile("v_mov_b32 %0, -1" : "=v"(o.x));
  asm volatile("v_mov_b32 %0, -1" : "=v"(o.y));
  asm volatile("v_mov_b32 %0, -1" : "=v"(o.z));
  asm volatile("v_mov_b32 %0, -1" : "=v"(o.w));
  return o;
}
__device__ __forceinline__ unsigned top(unsigned m, u32x4 v) { return max(max(m, max(v.x, v.y)), max(v.z, v.w)); }
__device__ __forceinline__ bool absent(unsigned m) { return __builtin_amdgcn_ballot_w64(m == 0xffffffffu) != 0; }
__device__ __forceinline__ void mac(f32x4& c, u32x4 a, u32x4 b, bool first) {
  if (first) asm("v_mfma_f32_16x16x32_bf16 %0, %1, %2, 0" : "=&v"(c) : "v"(a), "v"(b));
  else asm("v_mfma_f32_16x16x32_bf16 %0, %1, %2, %0" : "+v"(c) : "v"(a), "v"(b));
}
#define MFMA_DONE() do { SCHED(); asm volatile("s_nop 7"); SCHED(); } while (0)
template <int C>
__device__ __forceinline__ float dpp(float v) { return __uint_as_float(__builtin_amdgcn_update_dpp(0, __float_as_uint(v), C, 0xf, 0xf, false)); }
template <int C>
__device__ __forceinline__ float dppadd(float v) { return v + dpp<C>(v); }
__device__ __forceinline__ float swap16add(float a, float b) {
  auto r = __builtin_amdgcn_permlane16_swap(__float_as_uint(a), __float_as_uint(b), false, false);
  return __uint_as_float(r[0]) + __uint_as_float(r[1]);
}
__device__ __forceinline__ float swap32add(float a, float b) {
  auto r = __builtin_amdgcn_permlane32_swap(__float_as_uint(a), __float_as_uint(b), false, false);
  return __uint_as_float(r[0]) + __uint_as_float(r[1]);
}
__device__ __forceinline__ float sum16(float v) {
  v = dppadd<0x128>(dppadd<0x124>(v));
  v = swap16add(v, v);
  return swap32add(v, v);
}
__device__ __forceinline__ float colsums(f32x4 a, f32x4 b, bool hi8) {
  const float t0 = swap32add(a[0], b[0]), t1 = swap32add(a[1], b[1]), t2 = swap32add(a[2], b[2]), t3 = swap32add(a[3], b[3]);
  const float u0 = swap16add(t0, t2), u1 = swap16add(t1, t3);
  return (hi8 ? u1 : u0) + dpp<0x128>(hi8 ? u0 : u1);
}
__device__ __forceinline__ float rowsums(const float (&s)[8], float e, bool hi8) {
  const float z = colsums(f32x4{s[0], s[1], s[2], s[3]}, f32x4{s[4], s[5], s[6], s[7]}, hi8) + e;
  return dppadd<0x141>(dppadd<0x4e>(dppadd<0xb1>(z)));
}
__device__ __forceinline__ void fst(char* p, u64 v) { __hip_atomic_store((gu64*)p, v, __ATOMIC_RELAXED, __HIP_MEMORY_SCOPE_AGENT); }
__device__ __forceinline__ void lds_wait(LAS char* p, unsigned v) {
  _Pragma("unroll 1") for (int it = 0; it < SPIN_CAP * 16; ++it) {
    if (__hip_atomic_load((LAS unsigned*)p, __ATOMIC_RELAXED, __HIP_MEMORY_SCOPE_WORKGROUP) == v) break;
    __builtin_amdgcn_s_sleep(1);
  }
  asm volatile("" ::: "memory");
}
__device__ __forceinline__ unsigned lds_add(LAS char* p) {
  asm volatile("s_waitcnt lgkmcnt(0)" ::: "memory");
  return __hip_atomic_fetch_add((LAS unsigned*)p, 1u, __ATOMIC_RELAXED, __HIP_MEMORY_SCOPE_WORKGROUP);
}
__device__ __forceinline__ void owner(int f, int& w, int& j) {
  w = (f * 43) >> 7;
  const int r = f - 3 * w;
  j = (w & 1) ? (r == 0 ? 2 : r - 1) : r;
}
constexpr int due(int j) { return (j * 101) / 89; }
constexpr int issued(int it) {
  int n = 0;
  for (int j = 0; j < 90; ++j) n += due(j) < it;
  return n;
}
constexpr int X_W2 = 5, X_OP = 80;
constexpr int extra(int it) { return (it < 36 && it % 6 == 2) + (it == X_W2) + (it == X_OP); }
constexpr int younger(int it) {
  int n = issued(it) - (it - 18) - 1;
  for (int s = due(it - 18); s < it; ++s) n += extra(s);
  return n;
}

#define STAMP(i) do { SCHED(); unsigned long long t_, c_; asm volatile("s_memrealtime %0\n\ts_memtime %1\n\ts_waitcnt lgkmcnt(0)" : "=s"(t_), "=s"(c_) :: "memory"); \
  if (fresh_lane() == 0) { *(LAS unsigned*)(L + L_STAMP + (w * 64 + (i)) * 4) = (unsigned)t_; *(LAS unsigned*)(L + L_STAMP + (w * 64 + 32 + (i)) * 4) = (unsigned)c_; } SCHED(); } while (0)

__global__ __launch_bounds__(768, 3) void fused_block(const u16* __restrict__ x, const u16* __restrict__ wre, const u16* __restrict__ wim,
                                                      const u16* __restrict__ dre, const u16* __restrict__ dim, const u16* __restrict__ w1,
                                                      const u16* __restrict__ w2, const u16* __restrict__ cvec, const u16* __restrict__ wmod,
                                                      char* __restrict__ ws, u16* __restrict__ out) {
  extern __shared__ __attribute__((aligned(16))) char smem[];
  LAS char* L = (LAS char*)smem;
  const unsigned lb = (unsigned)(size_t)L;
  const int w = __builtin_amdgcn_readfirstlane(threadIdx.x >> 6);
  const int b = blockIdx.x, x8 = b & 7, idx = b >> 3, g = idx & 3, j8 = idx >> 2;
  const int mat = x8 >> 2, rta = 8 * (x8 & 3) + j8;
  const int kq4 = x8 >> 1, kh = kq4 & 1, R = 8 * (x8 & 1) + j8;
  const int rt = 2 * R + (g >> 1), sl = 2 * kq4 + (g & 1), pc = rt * 8 + sl;
  const int si = rt & 15;
  const int kq = w & 3, cg = w >> 2, crot = CROT == 1 ? g : (CROT == 2 ? j8 : (CROT == 3 ? j8 + (g >> 1) * 2 : 0));
  const bool job = idx < 27;
  const rsrc_t rWs = mkrs(ws, WS_END);
  const u32x4 sWs = mksrd(ws, WS_END), sS = mksrd(mat ? wim : wre, S * S * 2);
  const int abown = AB_OFF + ((mat * 4 + g) * 32 + rta) * 18432;
  *(LAS unsigned*)(L + L_STAMP + threadIdx.x * 4) = 0;
  if (threadIdx.x == 0) *(LAS u32x4*)(L + C_MP) = u32x4{0u, 0u, 0u, 0u};
  BAR();
  STAMP(0);
  auto ws_dma = [&](auto NT, int c, int n, int vdma) {
    dma<NT>(lb + (c & 1) * 65536 + n * 1024, vdma, sS, (128 * R + 8 * (n & 15)) * 4096 + kh * 2048 + ((c + crot) & 3) * 512 + (n >> 4) * 128);
  };
  auto mk_vdma = [&]() {
    const int lane = fresh_lane();
    return (lane >> 3) * 4096 + (((lane & 7) ^ ((4 * (w & 1) + (lane >> 4)) & 7)) * 16);
  };
  u64 tag;
  auto get_tag = [&]() {
    const u32x2 t = *(LAS u32x2*)(L + L_CT);
    tag = ((u64)(unsigned)__builtin_amdgcn_readfirstlane(t.y) << 32) | (unsigned)__builtin_amdgcn_readfirstlane(t.x);
  };
  auto await = [&](u32x2& v, int voff) {
    int n = SPIN_CAP;
    asm volatile(
        "1:\n\tv_cmp_ne_u64 vcc, %[tg], %[v]\n\ts_cbranch_vccz 2f\n\ts_sleep 2\n\tbuffer_load_dwordx2 %[v], %[vo], %[rs], 0 offen sc1\n\t"
        "s_sub_u32 %[n], %[n], 1\n\ts_waitcnt vmcnt(0)\n\ts_cbranch_scc0 1b\n2:"
        : [v] "+v"(v), [n] "+s"(n)
        : [tg] "s"(tag), [vo] "v"(voff), [rs] "s"(sWs)
        : "vcc", "scc", "memory");
  };
  u32x4 V[9], Wd[18], cw;
  u32x2 ev;
  unsigned cx;
  const bool mjob = b < 36;
  const u32x4 sC = mksrd(cvec, D * 2), sD = mksrd(mat ? dim : dre, D * D * 2);
  const int wr = (w + 2 * g) & 7;
  unsigned wm;
  __builtin_amdgcn_s_setprio(2);
  if (w >= 4) {
    const int lane = fresh_lane();
    const u32x4 sX = mksrd(x, S * D * 2);
    const int so = (64 * rta + 8 * wr) * 1152;
#pragma unroll
    for (int r = 0; r < 8; ++r) fill<XP>(V[r], lane * 16, sX, so + r * 1152);
    fill<XP>(V[8], (lane >> 3) * 1152 + 1024 + (lane & 7) * 16, sX, so);
  } else {
    const int lane = fresh_lane(), rb = 288 * (b & 1) + 72 * w;
    fill64<1>(ev, 0, sWs, E_OFF);
    asm volatile("buffer_load_dword %0, %1, %2, %3 offen sc1" : "=v"(wm) : "v"(TCH && idx >= 5 && w ? lane * 128 : OOB), "s"(sD), "s"(((idx - 5) * 3 + w - 1) * 8192) : "memory");
    if (mjob) {
      const u32x4 sM = mksrd(wmod, D * 6 * D * 2);
      const int so = rb * 6912 + (b >> 1) * 128;
#pragma unroll
      for (int i = 0; i < 8; ++i) fill<2>(V[i], (lane >> 3) * 8 * 6912 + (lane & 7) * 16, sM, so + i * 6912);
      fill<2>(V[8], (lane >> 3) * 6912 + (lane & 7) * 16, sM, so + 64 * 6912);
      fill<0>(cw, (lane >> 3) * 16, sC, rb * 2);
      asm volatile("buffer_load_ushort %0, %1, %2, %3 offen" : "=v"(cx) : "v"((lane >> 3) * 2), "s"(sC), "s"(rb * 2 + 128) : "memory");
    }
  }
  __builtin_amdgcn_s_setprio(0);
  STAMP(6);
  auto wd_dma = [&](int q0, auto NQ) {
    constexpr int nq = NQ;
    const int lane = fresh_lane(), f = 9 * g + (w < 8 ? w : 8);
    const int vo = (8 * (lane >> 5) + ((lane >> 3) & 3)) * 1152 + (((lane & 7) ^ (lane >> 4)) * 16);
    const int so = (32 * (f >> 1) + 4 * (f & 1)) * 1152 + q0 * 128;
#pragma unroll
    for (int i = 0; i < nq; ++i) {
      dma<WDP == 2>(lb + w * 10240 + i * 2048, vo, sD, so + i * 128);
      dma<WDP == 2>(lb + w * 10240 + i * 2048 + 1024, vo ^ 64, sD, so + i * 128 + 16 * 1152);
    }
  };
  auto wd_get = [&](auto K0, auto N) {
    constexpr int k0 = K0, n = N;
    const int lane = fresh_lane();
    const int p = w * 10240 + (lane & 15) * 128 + (((lane >> 4) ^ ((lane >> 1) & 7)) * 16);
    const int pe = w >= 10 ? p ^ 64 : p, po = w >= 10 ? p + 2048 : p ^ 64;
#pragma unroll
    for (int i = 0; i < n; ++i) Wd[k0 + i] = *(LAS u32x4*)(L + ((i & 1) ? po : pe) + (i >> 1) * 2048);
#pragma unroll
    for (int i = 0; i < n; ++i) PIN(Wd[k0 + i]);
  };
  wd_dma(w >= 10 ? 4 : 0, ic<5>{});
  STAMP(7);
  asm volatile("s_waitcnt vmcnt(10)"
               : "+v"(V[0]), "+v"(V[1]), "+v"(V[2]), "+v"(V[3]), "+v"(V[4]), "+v"(V[5]), "+v"(V[6]), "+v"(V[7]), "+v"(V[8]), "+v"(ev), "+v"(cw),
                 "+v"(cx), "+v"(wm));
  STAMP(1);
  auto silu = [&](float c) { return rbf(c / (1.f + __expf(-c))); };
  if (w >= 4) {
    const int lane = fresh_lane();
    float s1[8], s2[8], e1, e2, pv = 0.f;
    auto sums = [&](u32x4 v, float p, float& a, float& q) {
      const f32x4 d0 = up4(v.x, v.y) - p, d1 = up4(v.z, v.w) - p, t = d0 + d1, u = d0 * d0 + d1 * d1;
      a = (t[0] + t[1]) + (t[2] + t[3]);
      q = (u[0] + u[1]) + (u[2] + u[3]);
    };
#pragma unroll
    for (int r = 0; r < 8; ++r) {
      const unsigned v0 = V[r].x;
      const float p = bflo(__builtin_amdgcn_readfirstlane(v0));
      sums(V[r], p, s1[r], s2[r]);
      pv = (lane >> 3) == r ? p : pv;
      PIN(V[r]);
      if (r & 1) SCHED();
    }
    sums(V[8], pv, e1, e2);
    PIN(V[8]);
    const float m = rowsums(s1, e1, lane & 8) * (1.f / D), var = rowsums(s2, e2, lane & 8) * (1.f / D) - m * m;
    float rs = rsqrtf(fmaxf(var, 0.f) + 1e-5f), sh = -(pv + m) * rs;
    PIN(rs);
    PIN(sh);
    STAMP(2);
    VMW(0);
    wd_get(ic<0>{}, ic<10>{});
    if (w < 8) {
      wd_dma(5, ic<4>{});
    } else {
      const u32x4 ones = blank();
      const int vl = (CLR & 4) ? fresh_lane() * 16 : OOB;
#pragma unroll
      for (int i = 0; i < 5; ++i) cst128(ones, rWs, w - 8 + 4 * i < 18 ? vl : OOB, abown + (w - 8 + 4 * i) * 1024);
    }
    STAMP(29);
#pragma unroll
    for (int r = 0; r < 9; ++r) {
      const float a = r < 8 ? __uint_as_float(__builtin_amdgcn_readlane(__float_as_uint(rs), 8 * (r & 7))) : rs;
      const float c = r < 8 ? __uint_as_float(__builtin_amdgcn_readlane(__float_as_uint(sh), 8 * (r & 7))) : sh;
      V[r] = pk8(up4(V[r].x, V[r].y) * a + c, up4(V[r].z, V[r].w) * a + c);
      PIN(V[r]);
    }
    if (w < 8) VMW(0);
  } else {
    const int lane = fresh_lane();
    tag = (((u64)(unsigned)__builtin_amdgcn_readfirstlane(ev.y) << 32) | (unsigned)__builtin_amdgcn_readfirstlane(ev.x)) + 1;
    const unsigned tl = (unsigned)tag;
    if (w == 0 && lane == 0) *(LAS u64*)(L + L_CT) = tag;
    bool last = false;
    f32x2 v;
    if (mjob) {
      f32x4 a0 = {0.f, 0.f, 0.f, 0.f}, a1 = a0;
#pragma unroll
      for (int i = 0; i < 9; ++i) {
        const unsigned u = i < 8 ? cw[i >> 1] : cx;
        const float sv = silu((i & 1) ? bfhi(u) : bflo(u));
        a0 += sv * up4(V[i].x, V[i].y);
        a1 += sv * up4(V[i].z, V[i].w);
      }
      *(LAS float*)(L + L_MP + w * 256 + (lane & 7) * 32 + (lane >> 3) * 4) = colsums(a0, a1, lane & 8);
      unsigned n = 0;
      if (lane == 0) n = lds_add(L + C_MP);
      last = __builtin_amdgcn_readfirstlane(n) == 3;
      asm volatile("" ::: "memory");
      LAS char* p = L + L_MP + (lane & 31) * 8;
      v = (*(LAS f32x2*)p + *(LAS f32x2*)(p + 256)) + (*(LAS f32x2*)(p + 512) + *(LAS f32x2*)(p + 768));
    }
#pragma unroll
    for (int i = 0; i < 4; ++i)
      cst128(u32x4{__float_as_uint(v.x), tl, __float_as_uint(v.y), tl}, rWs, last ? (lane >> 5) * 18432 + (lane & 31) * 16 : OOB,
             MG_OFF + i * 36864 + (b & 1) * 9216 + (b >> 1) * 512);
    STAMP(2);
    VMW(4);
    wd_get(ic<0>{}, ic<10>{});
    wd_dma(5, ic<4>{});
    STAMP(29);
    const int mo = MG_OFF + x8 * 18432 + w * 1024, vl = lane * 16, v2 = w == 0 ? vl : OOB;
    u32x4 p[6];
    _Pragma("unroll 1") for (int it = 0; it < SPIN_CAP; ++it) {
#pragma unroll
      for (int i = 0; i < 6; ++i) fill<1>(p[i], i < 4 ? vl : v2, sWs, mo + (i >> 1) * 4096 + (i & 1) * 9216);
      asm volatile("s_waitcnt vmcnt(0)" : "+v"(p[0]), "+v"(p[1]), "+v"(p[2]), "+v"(p[3]), "+v"(p[4]), "+v"(p[5]));
      unsigned d = 0, e = 0;
#pragma unroll
      for (int i = 0; i < 4; ++i) d |= (p[i].y ^ tl) | (p[i].w ^ tl);
#pragma unroll
      for (int i = 4; i < 6; ++i) e |= (p[i].y ^ tl) | (p[i].w ^ tl);
      if (__builtin_amdgcn_ballot_w64((w == 0 ? d | e : d) != 0) == 0) break;
      __builtin_amdgcn_s_sleep(2);
    }
#pragma unroll
    for (int i = 0; i < 3; ++i) {
      unsigned o = pk(__uint_as_float(p[2 * i].x) + __uint_as_float(p[2 * i + 1].x), __uint_as_float(p[2 * i].z) + __uint_as_float(p[2 * i + 1].z));
      if ((w + 4 * i) * 64 + lane >= 288) o = pk(bflo(o) + 1.f, bfhi(o) + 1.f);
      if (i < 2 || w == 0) *(LAS f32x2*)(L + L_SA + (w + 4 * i) * 512 + lane * 8) = f32x2{bflo(o), bfhi(o)};
    }
  }
  STAMP(30);
  u32x4 Wr[6];
  u32x3 cv3;
  const int bn = idx * 12 + w, bph = bn >= 162, bkb = ((bn - 162 * bph) * 57) >> 9, bnb = bn - 162 * bph - 9 * bkb, jm = x8 * 5 + idx - 27;
  {
    const int lane = fresh_lane();
    const int pitch = job ? (bph ? 1152 : F * 2) : 6912, step = job ? 8 * pitch : pitch;
    const int so = job ? (32 * bkb + (bph ? x8 * 576 : 0)) * pitch + (bph ? 0 : x8 * 1152) + bnb * 128 : 48 * w * 6912 + 2304 + jm * 128;
    const u32x4 sW = mksrd(job ? (bph ? w2 : w1) : wmod, job ? D * F * 2 : (jm < 36 ? D * 6 * D * 2 : 0u));
    const int vo = (lane >> 3) * (job ? pitch : 6 * 6912) + (lane & 7) * 16;
#pragma unroll
    for (int i = 0; i < 6; ++i) fill<2>(Wr[i], i < 4 || !job ? vo : OOB, sW, so + i * step);
    asm volatile("buffer_load_dwordx3 %0, %1, %2, %3 offen" : "=v"(cv3) : "v"((lane >> 3) * 12), "s"(sC), "s"(96 * w) : "memory");
  }
  STAMP(31);
  if (w < 8) wd_get(ic<10>{}, ic<8>{});
  BAR();
  STAMP(3);
  u32x2 gv;
  if (w >= 4) {
    get_tag();
    const int lane = fresh_lane();
    LAS char* sp = L + L_SA + lane * 32;
    LAS char* sq = L + L_SA + 2048 + (lane & 7) * 32;
    const f32x4 sa0 = *(LAS f32x4*)sp, sa1 = *(LAS f32x4*)(sp + 16), ca0 = *(LAS f32x4*)(sp + 2304), ca1 = *(LAS f32x4*)(sp + 2320);
    const int hb = L_H + (wr >> 2) * 2048 + (lane & 3) * 256 + (((4 * wr) ^ (lane & 7)) & 15) * 16;
#pragma unroll
    for (int r = 0; r < 8; ++r) {
      *(LAS u32x4*)(L + ((hb + (lane >> 2) * 4096) ^ ((r & 3) * 16)) + (r >> 2) * 1024) = pk8(up4(V[r].x, V[r].y) * ca0 + sa0, up4(V[r].z, V[r].w) * ca1 + sa1);
      if (r & 1) SCHED();
    }
    const f32x4 ta0 = *(LAS f32x4*)sq, ta1 = *(LAS f32x4*)(sq + 16), da0 = *(LAS f32x4*)(sq + 2304), da1 = *(LAS f32x4*)(sq + 2320);
    *(LAS u32x4*)(L + ((hb + 65536 + (lane & 4) * 1024 + (lane >> 5) * 1024) ^ (((lane >> 3) & 3) * 16))) =
        pk8(up4(V[8].x, V[8].y) * da0 + ta0, up4(V[8].z, V[8].w) * da1 + ta1);
    VMW(7);
  } else {
    const int vdma = mk_vdma();
#pragma unroll
    for (int i = 0; i < 16; ++i) ws_dma(std::integral_constant<bool, C0NT>{}, 0, w + 4 * i, vdma);
  }
  BAR();
  STAMP(4);
  {
    const int lane = fresh_lane();
    const int ra = L_H + (lane >> 4) * 256 + (((lane & 15) ^ (lane >> 4)) * 16);
    const unsigned neg = mat ? 0x80008000u : 0u;
    int fw, fj;
    owner(9 * g + (w < 8 ? w : 8), fw, fj);
    const int so = abown + (fw - 3 * g) * 6144 + fj * 1024;
    f32x4 c[4];
    if (w < 8) {
      if (w == 4 && lane == 0) fst(ws + G0_OFF + ((mat * 4 + g) * 32 + rta) * 8, tag);
      u32x4 Hf[2][4];
#pragma unroll
      for (int rb = 0; rb < 4; ++rb) Hf[0][rb] = *(LAS u32x4*)(L + ra + rb * 1024);
      sfor<18>([&](auto KB) {
        constexpr int kb = KB;
        if (kb < 17) {
#pragma unroll
          for (int rb = 0; rb < 4; ++rb) Hf[(kb + 1) & 1][rb] = *(LAS u32x4*)(L + (ra ^ ((kb + 1) & 1) * 64) + (kb + 1) * 4096 + rb * 1024);
        }
        SCHED();
#pragma unroll
        for (int rb = 0; rb < 4; ++rb) mac(c[rb], Hf[kb & 1][rb], Wd[kb], kb == 0);
        SCHED();
      });
      MFMA_DONE();
      cst128(live(pk8(c[0], c[1]) ^ neg), rWs, lane * 16, so);
      cst128(live(pk8(c[2], c[3]) ^ neg), rWs, lane * 16, so + 3072);
      VMW_SEL(2, 16, 4, );
    } else {
      const int g0 = G0_OFF + ((mat * 4 + g) * 32 + 16 * kh + (lane & 15)) * 8;
      fill64<1>(gv, w == 11 ? g0 : OOB, sWs, 0);
      const int rk = (ra ^ (w >= 10 ? 64 : 0)) + (w >= 10 ? 9 * 4096 : 0) + (w & 1) * 2048;
      u32x4 Hf[4][2];
      auto ldh = [&](int i) {
        Hf[i & 3][0] = *(LAS u32x4*)(L + (rk ^ (i & 1) * 64) + i * 4096);
        Hf[i & 3][1] = *(LAS u32x4*)(L + (rk ^ (i & 1) * 64) + i * 4096 + 1024);
      };
      ldh(0);
      ldh(1);
      ldh(2);
#pragma unroll
      for (int i = 0; i < 9; ++i) {
        if (i < 6) ldh(i + 3);
        SCHED();
        mac(c[0], Hf[i & 3][0], Wd[i], i == 0);
        mac(c[1], Hf[i & 3][1], Wd[i], i == 0);
        SCHED();
      }
      MFMA_DONE();
      LAS char* pp = L + L_PR + (w & 1) * 2048 + lane * 16;
      if (w >= 10) {
        *(LAS f32x4*)pp = c[0];
        *(LAS f32x4*)(pp + 1024) = c[1];
        if (lane == 0) lds_add(L + C_PR + (w & 1) * 4);
        asm volatile("s_waitcnt vmcnt(0)" : "+v"(gv));
        if (w == 11) await(gv, g0);
      } else {
        lds_wait(L + C_PR + (w & 1) * 4, 1);
        cst128(live(pk8(c[0] + *(LAS f32x4*)pp, c[1] + *(LAS f32x4*)(pp + 1024)) ^ neg), rWs, lane * 16, so + (w & 1) * 3072);
        asm volatile("s_waitcnt vmcnt(1)" : "+v"(gv));
      }
    }
  }
#pragma unroll
  for (int i = 0; i < 6; ++i) PIN(Wr[i]);
  PIN(cv3);
  STAMP(5);
  BAR();
  int vl16, rd0;
  {
    const int lane = fresh_lane();
    vl16 = lane * 16;
    rd0 = kq * 16384 + (lane & 15) * 128 + (((lane >> 4) ^ ((lane & 15) >> 1)) * 16);
    LAS char* sp = L + L_H + w * 4096;
    if (job) {
#pragma unroll
      for (int i = 0; i < 4; ++i) *(LAS u32x4*)(sp + i * 1024 + vl16) = Wr[i];
      LAS char* rp = sp + ((lane >> 4) * 8 + ((lane >> 2) & 3)) * 128 + (lane & 3) * 16;
#pragma unroll
      for (int i = 0; i < 4; ++i) {
        const u32x2 lo = __builtin_bit_cast(u32x2, __builtin_amdgcn_ds_read_tr16_b64_v4i16((LAS s16x4*)(rp + (i >> 1) * 64 + (i & 1) * 8)));
        const u32x2 hi = __builtin_bit_cast(u32x2, __builtin_amdgcn_ds_read_tr16_b64_v4i16((LAS s16x4*)(rp + (i >> 1) * 64 + (i & 1) * 8 + 512)));
        Wr[i] = u32x4{lo.x, lo.y, hi.x, hi.y};
      }
    } else {
      f32x4 a0 = {0.f, 0.f, 0.f, 0.f}, a1 = a0;
#pragma unroll
      for (int i = 0; i < 6; ++i) {
        const unsigned u = cv3[i >> 1];
        const float sv = silu((i & 1) ? bfhi(u) : bflo(u));
        a0 += sv * up4(Wr[i].x, Wr[i].y);
        a1 += sv * up4(Wr[i].z, Wr[i].w);
      }
      *(LAS float*)(sp + (lane & 7) * 32 + (lane >> 3) * 4) = colsums(a0, a1, lane & 8);
      unsigned n = 0;
      if (lane == 0) n = lds_add(L + C_MV);
      const bool last = __builtin_amdgcn_readfirstlane(n) == 11;
      asm volatile("" ::: "memory");
      f32x4 v = {0.f, 0.f, 0.f, 0.f};
#pragma unroll
      for (int k = 0; k < 12; ++k) v += *(LAS f32x4*)(L + L_H + k * 4096 + (lane & 15) * 16);
      u32x2 o = pk4(v);
      if (jm >= 18 && jm < 27) o = pk4(up4(o.x, o.y) + 1.f);
      cst128(u32x4{o.x, (unsigned)tag, o.y, (unsigned)tag}, rWs, last && jm < 36 && lane < 16 ? vl16 : OOB, MV_OFF + jm * 256);
    }
  }
  const int abin = AB_OFF + ((mat * 4 + g) * 32 + 16 * kh + kq) * 18432 + cg * 6144;
  auto ab_at = [&](int c) { return abin + ((c + crot) & 3) * 73728; };
  u32x4 Ra[6];
  _Pragma("unroll 1") for (int it = 0; it < SPIN_CAP; ++it) {
#pragma unroll
    for (int i = 0; i < 6; ++i) fill<1>(Ra[i], vl16, sWs, ab_at(0) + i * 1024);
    asm volatile("s_waitcnt vmcnt(0)" : "+v"(Ra[0]), "+v"(Ra[1]), "+v"(Ra[2]), "+v"(Ra[3]), "+v"(Ra[4]), "+v"(Ra[5]));
    unsigned m = 0;
#pragma unroll
    for (int i = 0; i < 6; ++i) m = top(m, Ra[i]);
    if (!absent(m)) break;
  }
  STAMP(8);
#pragma unroll
  for (int i = 0; i < 4; ++i) {
    int fw, fj;
    owner(4 * bnb + i, fw, fj);
    cst128(Wr[i], rWs, job ? vl16 : OOB, WT_OFF + (x8 * 12 + fw) * 110592 + (bph * 54 + bkb * 3 + fj) * 1024);
  }
  BAR();
  STAMP(9);
  f32x4 acc[8][3];
  auto touch = [&](unsigned& r, int ph) {
    const int lane = fresh_lane();
    asm volatile("buffer_load_dword %0, %1, %2, %3 offen sc1"
                 : "=v"(r)
                 : "v"(lane < 27 && (T0 || ph) ? lane * 128 : OOB), "s"(sWs), "s"(WT_OFF + (sl * 12 + w) * 110592 + ph * 55296 + si * 3456)
                 : "memory");
  };
  {
    u32x4 Wf[2][4];
    auto ldw = [&](int s, int h) {
#pragma unroll
      for (int r = 0; r < 4; ++r) Wf[h][r] = *(LAS u32x4*)(L + ((s >> 1) & 1) * 65536 + (4 * h + r) * 2048 + (rd0 ^ ((s & 1) * 64)));
    };
    sfor<8>([&](auto SS) {
      constexpr int s = SS, c = s >> 1, kk = s & 1;
      if constexpr (s >= 2) {
        constexpr int y = s == 7 ? 1 : (s == 6 ? 3 : (kk ? 9 : 4));
        asm volatile("s_waitcnt vmcnt(%3)" : "+v"(Ra[3 * kk]), "+v"(Ra[3 * kk + 1]), "+v"(Ra[3 * kk + 2]) : "i"(y));
      }
      if constexpr (kk == 0) {
        if constexpr (s > 0) BAR();
        STAMP(10 + c);
        ldw(s, 0);
        SCHED();
        if constexpr (c < 3) {
          const int vdma = mk_vdma();
#pragma unroll
          for (int i = 0; i < 5; ++i) ws_dma(std::false_type{}, c + 1, w + 12 * i, vdma);
          if (w < 4) ws_dma(std::false_type{}, c + 1, 60 + w, vdma);
        }
        if constexpr (s == 2) {
          if (w == 11 && fresh_lane() == 0) fst(ws + GW_OFF + (x8 * 32 + idx) * 8, tag);
        }
        if constexpr (s == 4) fill64<1>(gv, GW_OFF + (sl * 32 + (fresh_lane() & 31)) * 8, sWs, 0);
        if constexpr (s == 6) {
          if (w == 11 && fresh_lane() == 0) fst(ws + G1_OFF + pc * 8, tag);
          await(gv, GW_OFF + (sl * 32 + (fresh_lane() & 31)) * 8);
          touch(wm, 0);
        }
      }
      if constexpr (s < 4) cst128(blank(), rWs, (CLR & 1) && (s < 3 || w < 9) ? vl16 : OOB, XB_OFF + pc * 46080 + (w + 12 * s) * 1024);
      if constexpr (s >= 2) {
        if (absent(top(top(top(0u, Ra[3 * kk]), Ra[3 * kk + 1]), Ra[3 * kk + 2]))) {
          _Pragma("unroll 1") for (int it = 0; it < SPIN_CAP; ++it) {
#pragma unroll
            for (int j = 0; j < 3; ++j) refill(Ra[3 * kk + j], vl16, sWs, ab_at(c) + kk * 3072 + j * 1024);
            asm volatile("s_waitcnt vmcnt(0)" : "+v"(Ra[3 * kk]), "+v"(Ra[3 * kk + 1]), "+v"(Ra[3 * kk + 2]));
            if (!absent(top(top(top(0u, Ra[3 * kk]), Ra[3 * kk + 1]), Ra[3 * kk + 2]))) break;
          }
        }
      }
      SCHED();
      ldw(s, 1);
      SCHED();
#pragma unroll
      for (int j = 0; j < 3; ++j)
#pragma unroll
        for (int r = 0; r < 4; ++r) mac(acc[r][j], Ra[3 * kk + j], Wf[0][r], s == 0);
      SCHED();
      if constexpr (kk == 0) ldw(s + 1, 0);
      SCHED();
#pragma unroll
      for (int j = 0; j < 3; ++j) {
#pragma unroll
        for (int r = 0; r < 4; ++r) mac(acc[4 + r][j], Ra[3 * kk + j], Wf[1][r], s == 0);
        SCHED();
        if constexpr (c < 3) refill(Ra[3 * kk + j], vl16, sWs, ab_at(c + 1) + kk * 3072 + j * 1024);
        SCHED();
      }
    });
  }
  MFMA_DONE();
  STAMP(14);
  auto fin_map = [&](int lane, int& flo, int& fhi, bool& alo, bool& ahi) {
    const int s = lane >> 4, f2 = 3 * w + ((w & 1) ? 0 : 2);
    flo = 8 * ((lane >> 2) & 3) + (s < 2 ? 32 * ((3 * w + 1) >> 1) : 32 * (f2 >> 1) + 4 * (f2 & 1));
    fhi = flo + (s < 2 ? 4 : 0);
    alo = s == 1;
    ahi = s >= 1;
  };
  const int row0 = 32 * (pc >> 2) + 4 * (pc & 3);
  u32x2 xa, xb;
  u32x4 mv;
  int gvo;
  {
    const int lane = fresh_lane();
    int flo, fhi;
    bool alo, ahi;
    fin_map(lane, flo, fhi, alo, ahi);
    const u32x4 sX = mksrd(x, S * D * 2);
    gvo = G1_OFF + (16 * R + (lane & 15)) * 8;
    fill64<1>(gv, gvo, sWs, 0);
    fill64<0>(xa, lane < 48 ? (16 * alo + (lane & 3)) * 1152 + flo * 2 : OOB, sX, row0 * 1152);
    fill64<0>(xb, lane < 48 ? (16 * ahi + (lane & 3)) * 1152 + fhi * 2 : OOB, sX, row0 * 1152);
    fill<1>(mv, w < 9 ? vl16 : OOB, sWs, MV_OFF + w * 1024);
  }
  BAR();
  f32x4 own[2][3];
#pragma unroll
  for (int d = 0; d < 4; ++d) {
    if (d != kq) {
      LAS char* p = L + L_RED + ((cg * 4 + d) * 3 + ((kq - d - 1) & 3)) * 3072 + vl16;
      *(LAS u32x4*)p = pk8(acc[2 * d][0], acc[2 * d][1]);
      *(LAS u32x4*)(p + 1024) = pk8(acc[2 * d + 1][0], acc[2 * d + 1][1]);
      *(LAS u32x4*)(p + 2048) = pk8(acc[2 * d][2], acc[2 * d + 1][2]);
    } else {
#pragma unroll
      for (int j = 0; j < 3; ++j) {
        own[0][j] = acc[2 * d][j];
        own[1][j] = acc[2 * d + 1][j];
      }
    }
  }
  BAR();
  STAMP(15);
  {
    LAS char* p = L + L_RED + w * 9216 + vl16;
#pragma unroll
    for (int s = 0; s < 3; ++s) {
      const u32x4 a = *(LAS u32x4*)(p + s * 3072), c = *(LAS u32x4*)(p + s * 3072 + 1024), e = *(LAS u32x4*)(p + s * 3072 + 2048);
      own[0][0] += up4(a.x, a.y);
      own[0][1] += up4(a.z, a.w);
      own[1][0] += up4(c.x, c.y);
      own[1][1] += up4(c.z, c.w);
      own[0][2] += up4(e.x, e.y);
      own[1][2] += up4(e.z, e.w);
    }
    asm volatile("s_waitcnt vmcnt(3)" : "+v"(gv), "+v"(wm));
    await(gv, gvo);
    const int lane = fresh_lane(), vpub = ((lane >> 2) & 3) * 46080 + (lane >> 4) * 64 + (lane & 3) * 16;
    const int yb = XB_OFF + (4 * R + kq) * 4 * 46080 + kq4 * 9216 + (3 * g + cg) * 768;
    cst128(live(pk8(own[0][0], own[0][1])), rWs, vpub, yb);
    cst128(live(pk8(own[1][0], own[1][1])), rWs, vpub, yb + 256);
    cst128(live(pk8(own[0][2], own[1][2])), rWs, vpub, yb + 512);
  }
  STAMP(16);
  asm volatile("s_waitcnt vmcnt(3)" : "+v"(xa), "+v"(xb), "+v"(mv));
  if (w < 9) {
    _Pragma("unroll 1") for (int it = 0; it < SPIN_CAP; ++it) {
      if (__builtin_amdgcn_ballot_w64(((mv.y ^ (unsigned)tag) | (mv.w ^ (unsigned)tag)) != 0) == 0) break;
      refill(mv, vl16, sWs, MV_OFF + w * 1024);
      asm volatile("s_waitcnt vmcnt(0)" : "+v"(mv));
    }
    *(LAS f32x4*)(L + L_MV + w * 1024 + vl16) = up4(mv.x, mv.z);
  }
  const int rot = ROT ? (si * 18) >> 4 : 0, wb = WT_OFF + (sl * 12 + w) * 110592;
  auto w_at = [&](int it) {
    const int ph = it / 54, k = (it - 54 * ph) / 3 + rot;
    return wb + ph * 55296 + (k >= 18 ? k - 18 : k) * 3072 + (it % 3) * 1024;
  };
  u32x4 Rw[18];
#pragma unroll
  for (int i = 0; i < 9; ++i) fill<1>(Rw[i], vl16, sWs, w_at(i));
  BAR();
  STAMP(17);
  {
    const int lane = fresh_lane(), v48 = lane < 48 ? vl16 : OOB;
    int flo, fhi;
    bool alo, ahi;
    fin_map(lane, flo, fhi, alo, ahi);
    u32x4 yp[4];
    _Pragma("unroll 1") for (int it = 0; it < SPIN_CAP; ++it) {
#pragma unroll
      for (int p = 0; p < 4; ++p) fill<1>(yp[p], v48, sWs, XB_OFF + pc * 46080 + p * 9216 + w * 768);
      asm volatile("s_waitcnt vmcnt(0)" : "+v"(yp[0]), "+v"(yp[1]), "+v"(yp[2]), "+v"(yp[3]));
      if (!absent(top(top(top(top(0u, yp[0]), yp[1]), yp[2]), yp[3]))) break;
      __builtin_amdgcn_s_sleep(2);
    }
    STAMP(18);
    const f32x4 gal = *(LAS f32x4*)(L + L_MV + flo * 4), gah = *(LAS f32x4*)(L + L_MV + fhi * 4);
    const f32x4 sbl = *(LAS f32x4*)(L + L_MV + 2304 + flo * 4), sbh = *(LAS f32x4*)(L + L_MV + 2304 + fhi * 4);
    const f32x4 cbl = *(LAS f32x4*)(L + L_MV + 4608 + flo * 4), cbh = *(LAS f32x4*)(L + L_MV + 4608 + fhi * 4);
    f32x4 yl = (up4(yp[0].x, yp[0].y) + up4(yp[1].x, yp[1].y)) + (up4(yp[2].x, yp[2].y) + up4(yp[3].x, yp[3].y));
    f32x4 yh = (up4(yp[0].z, yp[0].w) + up4(yp[1].z, yp[1].w)) + (up4(yp[2].z, yp[2].w) + up4(yp[3].z, yp[3].w));
    const u32x4 x2 = pk8(up4(xa.x, xa.y) + gal * yl, up4(xb.x, xb.y) + gah * yh);
    yl = up4(x2.x, x2.y);
    yh = up4(x2.z, x2.w);
    {
      const bool on = lane < 48;
      const float lo = (yl[0] + yl[1]) + (yl[2] + yl[3]), hi = (yh[0] + yh[1]) + (yh[2] + yh[3]);
      const float ma = sum16((alo ? 0.f : lo) + (ahi ? 0.f : hi)) * (1.f / 48), mb = sum16((alo ? lo : 0.f) + (ahi ? hi : 0.f)) * (1.f / 48);
      const f32x4 dl = yl - (alo ? mb : ma), dh = yh - (ahi ? mb : ma), ql = dl * dl, qh = dh * dh;
      const float q0 = on ? (ql[0] + ql[1]) + (ql[2] + ql[3]) : 0.f, q1 = on ? (qh[0] + qh[1]) + (qh[2] + qh[3]) : 0.f;
      const float qa = sum16((alo ? 0.f : q0) + (ahi ? 0.f : q1)), qb = sum16((alo ? q0 : 0.f) + (ahi ? q1 : 0.f));
      if (lane < 4) *(LAS f32x4*)(L + L_ST + (w * 4 + lane) * 16) = f32x4{ma, mb, qa, qb};
    }
    BAR();
    STAMP(19);
    f32x4 t[12];
#pragma unroll
    for (int k = 0; k < 12; ++k) t[k] = *(LAS f32x4*)(L + L_ST + (k * 4 + (lane & 3)) * 16);
    float ma = 0.f, mb = 0.f, va = 0.f, vb = 0.f, da = 0.f, db = 0.f;
#pragma unroll
    for (int k = 0; k < 12; ++k) {
      ma += t[k][0];
      mb += t[k][1];
      va += t[k][2];
      vb += t[k][3];
    }
    ma *= 1.f / 12;
    mb *= 1.f / 12;
#pragma unroll
    for (int k = 0; k < 12; ++k) {
      da += (t[k][0] - ma) * (t[k][0] - ma);
      db += (t[k][1] - mb) * (t[k][1] - mb);
    }
    va = rsqrtf((va + 48.f * da) * (1.f / D) + 1e-5f);
    vb = rsqrtf((vb + 48.f * db) * (1.f / D) + 1e-5f);
    cst128(live(pk8((yl - (alo ? mb : ma)) * (alo ? vb : va) * cbl + sbl, (yh - (ahi ? mb : ma)) * (ahi ? vb : va) * cbh + sbh)), rWs, v48,
           XB_OFF + pc * 46080 + 36864 + w * 768);
    if (w == 11 && b == 0 && lane == 0) fst(ws + E_OFF, tag);
#pragma unroll
    for (int i = 9; i < 18; ++i) fill<1>(Rw[i], vl16, sWs, w_at(i));
    if (lane < 48) *(LAS u32x4*)(L + L_X2 + w * 768 + vl16) = x2;
    STAMP(20);
    const int kp = (flo >> 5) - rot, lg = (lane >> 2) & 3;
    const int hl = (kp < 0 ? kp + 18 : kp) * 4096 + lg * 256 + (lg & 1) * 192 + (lane & 3) * 16 + (flo & 4) * 2;
    const int olo = hl + alo * 1024, ohi = hl + ahi * 1024 + (fhi - flo) * 2;
    u32x4 h[8];
    _Pragma("unroll 1") for (int it = 0; it < SPIN_CAP; ++it) {
#pragma unroll
      for (int p = 0; p < 8; ++p) fill<1>(h[p], v48, sWs, XB_OFF + (rt * 8 + p) * 46080 + 36864 + w * 768);
      asm volatile("s_waitcnt vmcnt(0)" : "+v"(h[0]), "+v"(h[1]), "+v"(h[2]), "+v"(h[3]), "+v"(h[4]), "+v"(h[5]), "+v"(h[6]), "+v"(h[7]));
      unsigned m = 0;
#pragma unroll
      for (int p = 0; p < 8; ++p) m = top(m, h[p]);
      if (!absent(m)) break;
      __builtin_amdgcn_s_sleep(2);
    }
    STAMP(21);
    if (lane < 48) {
#pragma unroll
      for (int p = 0; p < 8; ++p) {
        *(LAS u32x2*)(L + (olo ^ ((p & 3) * 64)) + (p >> 2) * 2048) = u32x2{h[p].x, h[p].y};
        *(LAS u32x2*)(L + (ohi ^ ((p & 3) * 64)) + (p >> 2) * 2048) = u32x2{h[p].z, h[p].w};
      }
    }
  }
#pragma unroll
  for (int i = 0; i < 18; ++i) PIN(Rw[i]);
  BAR();
  STAMP(22);
  f32x4 am[4][3];
  {
    sfor<2>([&](auto PH) {
      constexpr int ph = PH;
      LAS char* A = L + ph * L_H1 + (vl16 ^ ((fresh_lane() & 16) * 12));
      u32x4 Bf[2][4];
#pragma unroll
      for (int mb = 0; mb < 4; ++mb) Bf[0][mb] = *(LAS u32x4*)(A + mb * 1024);
      sfor<18>([&](auto KB) {
        constexpr int kb = KB;
        if (kb < 17) {
#pragma unroll
          for (int mb = 0; mb < 4; ++mb) Bf[(kb + 1) & 1][mb] = *(LAS u32x4*)(A + ((kb + 1) * 4 + mb) * 1024);
        }
        SCHED();
        sfor<3>([&](auto J) {
          constexpr int j = J, it = ph * 54 + kb * 3 + j;
          if constexpr (it >= 18) asm volatile("s_waitcnt vmcnt(%1)" : "+v"(Rw[it % 18]) : "i"(younger(it)));
#pragma unroll
          for (int mb = 0; mb < 4; ++mb) mac(am[mb][j], Rw[it % 18], Bf[kb & 1][mb], kb == 0);
          SCHED();
          constexpr int j0 = issued(it), j1 = issued(it + 1);
          sfor<j1 - j0>([&](auto JJ) {
            constexpr int jj = j0 + JJ;
            static_assert(jj <= it, "slot still live");
            refill(Rw[jj % 18], vl16, sWs, w_at(jj + 18));
          });
          if constexpr (it < 36 && it % 6 == 2) cst128(blank(), rWs, (CLR & 2) ? vl16 : OOB, OP_OFF + pc * 73728 + (w + 12 * (it / 6)) * 1024);
          if constexpr (it == X_W2) touch(wm, 1);
          if constexpr (it == 53) PIN(wm);
          if constexpr (it == X_OP) fill64<1>(gv, G3_OFF + (rt * 8 + (fresh_lane() & 7)) * 8, sWs, 0);
          SCHED();
        });
      });
      MFMA_DONE();
      STAMP(23 + 2 * ph);
      if constexpr (ph == 0) {
        const int fp = ((3 * w + 1) >> 1) - rot, fh = ((3 * (w & ~1) + 2) >> 1) - rot;
        LAS char* Hp = A + L_H1 + (fp < 0 ? fp + 18 : fp) * 4096;
        LAS char* Hh = A + L_H1 + (fh < 0 ? fh + 18 : fh) * 4096 + (w & 1) * 8;
#pragma unroll
        for (int mb = 0; mb < 4; ++mb) {
          *(LAS u32x4*)(Hp + mb * 1024) = pk8(relu4(am[mb][0]), relu4(am[mb][1]));
          *(LAS u32x2*)(Hh + mb * 1024) = pk4(relu4(am[mb][2]));
        }
        BAR();
        if (w == 0 && fresh_lane() == 0) fst(ws + G3_OFF + pc * 8, tag);
        STAMP(24);
      }
    });
  }
  {
    const int lane = fresh_lane(), v48 = lane < 48 ? vl16 : OOB;
    await(gv, G3_OFF + (rt * 8 + (lane & 7)) * 8);
    const int vp2 = ((lane >> 2) & 3) * 73728 + (lane >> 4) * 64 + (lane & 3) * 16;
#pragma unroll
    for (int h = 0; h < 2; ++h) {
      const int ob = OP_OFF + ((rt * 8 + 4 * h) * 8 + sl) * 9216 + w * 768;
      cst128(live(pk8(am[2 * h][0], am[2 * h][1])), rWs, vp2, ob);
      cst128(live(pk8(am[2 * h + 1][0], am[2 * h + 1][1])), rWs, vp2, ob + 256);
      cst128(live(pk8(am[2 * h][2], am[2 * h + 1][2])), rWs, vp2, ob + 512);
    }
    STAMP(26);
    BAR();
    int flo, fhi;
    bool alo, ahi;
    fin_map(lane, flo, fhi, alo, ahi);
    const u32x4 x2 = *(LAS u32x4*)(L + L_X2 + w * 768 + vl16);
    const f32x4 gl = *(LAS f32x4*)(L + L_GB + flo * 4), gh = *(LAS f32x4*)(L + L_GB + fhi * 4);
    u32x4 p[8];
    _Pragma("unroll 1") for (int it = 0; it < SPIN_CAP; ++it) {
#pragma unroll
      for (int k = 0; k < 8; ++k) fill<1>(p[k], v48, sWs, OP_OFF + (pc * 8 + k) * 9216 + w * 768);
      asm volatile("s_waitcnt vmcnt(0)" : "+v"(p[0]), "+v"(p[1]), "+v"(p[2]), "+v"(p[3]), "+v"(p[4]), "+v"(p[5]), "+v"(p[6]), "+v"(p[7]));
      unsigned m = 0;
#pragma unroll
      for (int k = 0; k < 8; ++k) m = top(m, p[k]);
      if (!absent(m)) break;
      __builtin_amdgcn_s_sleep(2);
    }
    STAMP(27);
    f32x4 sl4 = {0.f, 0.f, 0.f, 0.f}, sh4 = {0.f, 0.f, 0.f, 0.f};
#pragma unroll
    for (int k = 0; k < 8; ++k) {
      sl4 += up4(p[k].x, p[k].y);
      sh4 += up4(p[k].z, p[k].w);
    }
    const rsrc_t rO = mkrs(out, S * D * 2);
    const int olo = lane < 48 ? (16 * alo + (lane & 3)) * 1152 + flo * 2 : OOB, ohi = lane < 48 ? (16 * ahi + (lane & 3)) * 1152 + fhi * 2 : OOB;
    __builtin_amdgcn_raw_buffer_store_b64(pk4(up4(x2.x, x2.y) + gl * sl4), rO, olo, row0 * 1152, 0);
    __builtin_amdgcn_raw_buffer_store_b64(pk4(up4(x2.z, x2.w) + gh * sh4), rO, ohi, row0 * 1152, 0);
  }
  STAMP(28);
  BAR();
  if ((b == 0 || b == 100 || b == 201) && threadIdx.x == 0) {
    auto T = [&](int wv, int i) { return *(LAS unsigned*)(L + L_STAMP + (wv * 64 + i) * 4) - *(LAS unsigned*)(L + L_STAMP); };
    printf("B%d: x%u b2 %u b3 %u ab %u y0 %u y1 %u ye %u m0 %u m1 %u end %u\n", b, T(11, 1), T(0, 3), T(0, 4), T(11, 5), T(0, 9), T(0, 11), T(11, 14), T(0, 22), T(0, 24), T(11, 28));
  }
}

extern "C" void kernel_launch(void* const* d_in, const int* in_sizes, int n_in, void* d_out, int out_size, void* d_ws, size_t ws_size,
                              hipStream_t stream) {
  (void)hipFuncSetAttribute((const void*)fused_block, hipFuncAttributeMaxDynamicSharedMemorySize, LDS_BYTES);
  fused_block<<<256, 768, LDS_BYTES, stream>>>((const u16*)d_in[0], (const u16*)d_in[1], (const u16*)d_in[2], (const u16*)d_in[3],
                                               (const u16*)d_in[4], (const u16*)d_in[6], (const u16*)d_in[7], (const u16*)d_in[8],
                                               (const u16*)d_in[9], (char*)d_ws, (u16*)d_out);
}
```

```cpp
#include <hip/hip_runtime.h>
#include <hip/hip_bf16.h>
#include <stdint.h>
#include <utility>

typedef unsigned short u16;
typedef unsigned long long u64;
typedef __attribute__((ext_vector_type(2))) float f32x2;
typedef __attribute__((ext_vector_type(4))) float f32x4;
typedef __attribute__((ext_vector_type(2))) __bf16 bf16x2;
typedef __attribute__((ext_vector_type(4))) short s16x4;
typedef __attribute__((ext_vector_type(2))) unsigned u32x2;
typedef __attribute__((ext_vector_type(3))) unsigned u32x3;
typedef __attribute__((ext_vector_type(4))) unsigned u32x4;
typedef __amdgpu_buffer_rsrc_t rsrc_t;
#define LAS __attribute__((address_space(3)))
#define LAG __attribute__((address_space(1)))
typedef LAG u64 gu64;

constexpr int S = 2048, D = 576, F = 4608;
#define WDP 0
#define ROT 1
#define CLR 7
#define EXP2 0
#define XP 1
#define CROT 1
#define C0NT 0
#define TCH 1
#define IPF 1
#define T0 1
constexpr unsigned E_OFF = 0, G0_OFF = 0x1000, G1_OFF = 0x2000, GW_OFF = 0x3000, G3_OFF = 0x5000;
constexpr unsigned MG_OFF = 0x8000;
constexpr unsigned MV_OFF = 0x2c000;
constexpr unsigned AB_OFF = 0x30000;
constexpr unsigned XB_OFF = AB_OFF + 256 * 18432;
constexpr unsigned WT_OFF = XB_OFF + 256 * 46080;
constexpr unsigned WS_END = WT_OFF + 96 * 110592;
constexpr int SC1 = 16;
constexpr int OOB = (int)0x80000000u;
constexpr int L_H = 65536, L_SA = 139264, L_MP = 143872, L_PR = 144384;
constexpr int L_H1 = 73728, L_MV = 140544, L_GB = 147456, L_X2 = 149760, L_CT = 158976, L_STAMP = 159232, L_ST = 162304;
constexpr int C_MP = L_CT + 16, C_PR = L_CT + 20, C_MV = L_CT + 28, C_Q = L_CT + 32, C_R = L_CT + 48;
constexpr int LDS_BYTES = 163840;
constexpr int SPIN_CAP = 1 << 16;

template <int N>
using ic = std::integral_constant<int, N>;
template <class Fn, int... I>
__device__ __forceinline__ void sfor_(Fn&& f, std::integer_sequence<int, I...>) { (f(std::integral_constant<int, I>{}), ...); }
template <int N, class Fn>
__device__ __forceinline__ void sfor(Fn&& f) { sfor_(f, std::make_integer_sequence<int, N>{}); }

__device__ __forceinline__ rsrc_t mkrs(const void* p, unsigned bytes) { return __builtin_amdgcn_make_buffer_rsrc((void*)p, 0, bytes, 0x00020000); }
__device__ __forceinline__ u32x4 mksrd(const void* p, unsigned bytes) {
  const u64 a = (u64)p;
  return u32x4{(unsigned)a, (unsigned)(a >> 32) & 0xffffu, bytes, 0x00020000u};
}
#define SCHED() __builtin_amdgcn_sched_barrier(0)
__device__ __forceinline__ void cst128(u32x4 d, rsrc_t r, int v, int s) {
  __builtin_amdgcn_raw_buffer_store_b128(d, r, v, s, SC1);
  SCHED();
  asm volatile("s_nop 1" ::: "memory");
  SCHED();
}
template <int P>
__device__ __forceinline__ void dma(unsigned lds, int voff, u32x4 srd, int soff) {
  if (P == 2) asm volatile("s_mov_b32 m0, %0\n\ts_nop 3\n\tbuffer_load_dwordx4 %1, %2, %3 offen sc1 lds" ::"s"(lds), "v"(voff), "s"(srd), "s"(soff) : "memory");
  else if (P == 1) asm volatile("s_mov_b32 m0, %0\n\ts_nop 3\n\tbuffer_load_dwordx4 %1, %2, %3 offen nt lds" ::"s"(lds), "v"(voff), "s"(srd), "s"(soff) : "memory");
  else asm volatile("s_mov_b32 m0, %0\n\ts_nop 3\n\tbuffer_load_dwordx4 %1, %2, %3 offen lds" ::"s"(lds), "v"(voff), "s"(srd), "s"(soff) : "memory");
}
template <int P>
__device__ __forceinline__ void fill(u32x4& r, int voff, u32x4 srd, int soff) {
  if (P == 1) asm volatile("buffer_load_dwordx4 %0, %1, %2, %3 offen sc1" : "=v"(r) : "v"(voff), "s"(srd), "s"(soff) : "memory");
  else if (P == 2) asm volatile("buffer_load_dwordx4 %0, %1, %2, %3 offen nt" : "=v"(r) : "v"(voff), "s"(srd), "s"(soff) : "memory");
  else asm volatile("buffer_load_dwordx4 %0, %1, %2, %3 offen" : "=v"(r) : "v"(voff), "s"(srd), "s"(soff) : "memory");
}
__device__ __forceinline__ void refill(u32x4& r, int voff, u32x4 srd, int soff) {
  asm volatile("buffer_load_dwordx4 %0, %1, %2, %3 offen sc1" : "+v"(r) : "v"(voff), "s"(srd), "s"(soff) : "memory");
}
template <int P>
__device__ __forceinline__ void fill64(u32x2& r, int voff, u32x4 srd, int soff) {
  if (P == 1) asm volatile("buffer_load_dwordx2 %0, %1, %2, %3 offen sc1" : "=v"(r) : "v"(voff), "s"(srd), "s"(soff) : "memory");
  else asm volatile("buffer_load_dwordx2 %0, %1, %2, %3 offen" : "=v"(r) : "v"(voff), "s"(srd), "s"(soff) : "memory");
}
#define BAR() asm volatile("s_waitcnt lgkmcnt(0)\n\ts_barrier" ::: "memory")
#define VMW(n) asm volatile("s_waitcnt vmcnt(%0)" ::"i"(n) : "memory")
#define VMW_SEL(n, d, lim, ...) \
  asm volatile("s_waitcnt vmcnt(%[hi])\n\ts_cmp_lt_u32 %[ww], %[li]\n\ts_cbranch_scc1 1f\n\ts_waitcnt vmcnt(%[lo])\n1:" \
               : __VA_ARGS__ : [lo] "i"(n), [hi] "i"((n) + (d)), [li] "i"(lim), [ww] "s"(w) : "scc", "memory")
#define PIN(x) asm volatile("" : "+v"(x))
__device__ __forceinline__ int fresh_lane() {
  int l;
  asm volatile("v_mbcnt_lo_u32_b32 %0, -1, 0\n\tv_mbcnt_hi_u32_b32 %0, -1, %0" : "=v"(l));
  return l;
}
__device__ __forceinline__ float bflo(unsigned u) { return __uint_as_float(u << 16); }
__device__ __forceinline__ float bfhi(unsigned u) { return __uint_as_float(u & 0xffff0000u); }
__device__ __forceinline__ unsigned pk(float a, float b) {
  f32x2 v = {a, b};
  return __builtin_bit_cast(unsigned, __builtin_convertvector(v, bf16x2));
}
__device__ __forceinline__ float rbf(float a) { return bflo(pk(a, 0.f)); }
__device__ __forceinline__ u32x2 pk4(f32x4 a) { return u32x2{pk(a[0], a[1]), pk(a[2], a[3])}; }
__device__ __forceinline__ u32x4 pk8(f32x4 a, f32x4 b) { return u32x4{pk(a[0], a[1]), pk(a[2], a[3]), pk(b[0], b[1]), pk(b[2], b[3])}; }
__device__ __forceinline__ f32x4 up4(unsigned a, unsigned b) { return f32x4{bflo(a), bfhi(a), bflo(b), bfhi(b)}; }
__device__ __forceinline__ f32x4 relu4(f32x4 a) { return f32x4{fmaxf(a[0], 0.f), fmaxf(a[1], 0.f), fmaxf(a[2], 0.f), fmaxf(a[3], 0.f)}; }
__device__ __forceinline__ float nudge(float a) { return __uint_as_float(__float_as_uint(a) + 0x8000u); }
__device__ __forceinline__ u32x4 even8(f32x4 a, f32x4 b) {
  return u32x4{pk(nudge(a[0]), a[1]), pk(nudge(a[2]), a[3]), pk(nudge(b[0]), b[1]), pk(nudge(b[2]), b[3])} & 0xfffffffeu;
}
__device__ __forceinline__ unsigned odd2(float a, float b) { return __builtin_amdgcn_perm(pk(a, b), __float_as_uint(a), 0x07060302u) | 1u; }
__device__ __forceinline__ u32x4 odd8(f32x4 a, f32x4 b) { return u32x4{odd2(a[0], a[1]), odd2(a[2], a[3]), odd2(b[0], b[1]), odd2(b[2], b[3])}; }
__device__ __forceinline__ u32x4 blank() {
  u32x4 o;
  asm volatile("v_mov_b32 %0, -1" : "=v"(o.x));
  asm volatile("v_mov_b32 %0, -1" : "=v"(o.y));
  asm volatile("v_mov_b32 %0, -1" : "=v"(o.z));
  asm volatile("v_mov_b32 %0, -1" : "=v"(o.w));
  return o;
}
__device__ __forceinline__ unsigned top(unsigned m, u32x4 v) { return max(max(m, max(v.x, v.y)), max(v.z, v.w)); }
__device__ __forceinline__ bool absent(unsigned m) { return __builtin_amdgcn_ballot_w64(m == 0xffffffffu) != 0; }
__device__ __forceinline__ void mac(f32x4& c, u32x4 a, u32x4 b, bool first) {
  if (first) asm("v_mfma_f32_16x16x32_bf16 %0, %1, %2, 0" : "=&v"(c) : "v"(a), "v"(b));
  else asm("v_mfma_f32_16x16x32_bf16 %0, %1, %2, %0" : "+v"(c) : "v"(a), "v"(b));
}
__device__ __forceinline__ void pickers(u32x4& lo, u32x4& hi) {
  const int l = fresh_lane();
  const unsigned v = (l >> 4) == ((l >> 2) & 3) ? 0x3f80u << ((l & 1) * 16) : 0u, d0 = (l & 2) ? 0u : v, d1 = (l & 2) ? v : 0u;
  lo = u32x4{d0, d1, 0u, 0u};
  hi = u32x4{0u, 0u, d0, d1};
  PIN(lo);
  PIN(hi);
  SCHED();
  asm volatile("s_nop 1");
  SCHED();
}
#define MFMA_DONE() do { SCHED(); asm volatile("s_nop 7"); SCHED(); } while (0)
template <int C>
__device__ __forceinline__ float dpp(float v) { return __uint_as_float(__builtin_amdgcn_update_dpp(0, __float_as_uint(v), C, 0xf, 0xf, false)); }
template <int C>
__device__ __forceinline__ float dppadd(float v) { return v + dpp<C>(v); }
__device__ __forceinline__ float swap16add(float a, float b) {
  auto r = __builtin_amdgcn_permlane16_swap(__float_as_uint(a), __float_as_uint(b), false, false);
  return __uint_as_float(r[0]) + __uint_as_float(r[1]);
}
__device__ __forceinline__ float swap32add(float a, float b) {
  auto r = __builtin_amdgcn_permlane32_swap(__float_as_uint(a), __float_as_uint(b), false, false);
  return __uint_as_float(r[0]) + __uint_as_float(r[1]);
}
__device__ __forceinline__ float sum16(float v) {
  v = dppadd<0x128>(dppadd<0x124>(v));
  v = swap16add(v, v);
  return swap32add(v, v);
}
__device__ __forceinline__ float colsums(f32x4 a, f32x4 b, bool hi8) {
  const float t0 = swap32add(a[0], b[0]), t1 = swap32add(a[1], b[1]), t2 = swap32add(a[2], b[2]), t3 = swap32add(a[3], b[3]);
  const float u0 = swap16add(t0, t2), u1 = swap16add(t1, t3);
  return (hi8 ? u1 : u0) + dpp<0x128>(hi8 ? u0 : u1);
}
__device__ __forceinline__ float rowsums(const float (&s)[8], float e, bool hi8) {
  const float z = colsums(f32x4{s[0], s[1], s[2], s[3]}, f32x4{s[4], s[5], s[6], s[7]}, hi8) + e;
  return dppadd<0x141>(dppadd<0x4e>(dppadd<0xb1>(z)));
}
__device__ __forceinline__ void fst(char* p, u64 v) { __hip_atomic_store((gu64*)p, v, __ATOMIC_RELAXED, __HIP_MEMORY_SCOPE_AGENT); }
__device__ __forceinline__ void lds_wait(LAS char* p, unsigned v) {
  _Pragma("unroll 1") for (int it = 0; it < SPIN_CAP * 16; ++it) {
    if (__hip_atomic_load((LAS unsigned*)p, __ATOMIC_RELAXED, __HIP_MEMORY_SCOPE_WORKGROUP) == v) break;
    __builtin_amdgcn_s_sleep(1);
  }
  asm volatile("" ::: "memory");
}
__device__ __forceinline__ unsigned lds_add(LAS char* p) {
  asm volatile("s_waitcnt lgkmcnt(0)" ::: "memory");
  return __hip_atomic_fetch_add((LAS unsigned*)p, 1u, __ATOMIC_RELAXED, __HIP_MEMORY_SCOPE_WORKGROUP);
}
__device__ __forceinline__ void owner(int f, int& w, int& j) {
  w = (f * 43) >> 7;
  const int r = f - 3 * w;
  j = (w & 1) ? (r == 0 ? 2 : r - 1) : r;
}
constexpr int due(int j) { return (j * 101) / 89; }
constexpr int issued(int it) {
  int n = 0;
  for (int j = 0; j < 90; ++j) n += due(j) < it;
  return n;
}
constexpr int X_W2 = 5, X_OP = 80;
constexpr int extra(int it) { return (it == X_W2) + (it == X_OP); }
constexpr int younger(int it) {
  int n = issued(it) - (it - 18) - 1;
  for (int s = due(it - 18); s < it; ++s) n += extra(s);
  return n;
}

#define STAMP(i)

__global__ __launch_bounds__(768, 3) void fused_block(const u16* __restrict__ x, const u16* __restrict__ wre, const u16* __restrict__ wim,
                                                      const u16* __restrict__ dre, const u16* __restrict__ dim, const u16* __restrict__ w1,
                                                      const u16* __restrict__ w2, const u16* __restrict__ cvec, const u16* __restrict__ wmod,
                                                      char* __restrict__ ws, u16* __restrict__ out) {
  extern __shared__ __attribute__((aligned(16))) char smem[];
  LAS char* L = (LAS char*)smem;
  const unsigned lb = (unsigned)(size_t)L;
  const int w = __builtin_amdgcn_readfirstlane(threadIdx.x >> 6);
  const int b = blockIdx.x, x8 = b & 7, idx = b >> 3, g = idx & 3, j8 = idx >> 2;
  const int mat = x8 >> 2, rta = 8 * (x8 & 3) + j8;
  const int kq4 = x8 >> 1, kh = kq4 & 1, R = 8 * (x8 & 1) + j8;
  const int rt = 2 * R + (g >> 1), sl = 2 * kq4 + (g & 1), pc = rt * 8 + sl;
  const int si = rt & 15;
  const int kq = w & 3, cg = w >> 2, crot = CROT == 1 ? g : (CROT == 2 ? j8 : (CROT == 3 ? j8 + (g >> 1) * 2 : (CROT == 4 ? g + j8 : 0)));
  const bool job = idx < 27;
  const rsrc_t rWs = mkrs(ws, WS_END);
  const u32x4 sWs = mksrd(ws, WS_END), sS = mksrd(mat ? wim : wre, S * S * 2);
  const int abown = AB_OFF + ((mat * 4 + g) * 32 + rta) * 18432;
  if (threadIdx.x < 3) *(LAS u32x4*)(L + C_MP + threadIdx.x * 16) = u32x4{0u, 0u, 0u, 0u};
  BAR();
  STAMP(0);
  auto ws_dma = [&](auto NT, int c, int n, int vdma) {
    dma<NT>(lb + (c & 1) * 65536 + n * 1024, vdma, sS, (128 * R + 8 * ((n & 15) ^ ((n >> 4) << 2))) * 4096 + kh * 2048 + ((c + crot) & 3) * 512 + (n >> 4) * 128);
  };
  auto mk_vdma = [&]() {
    const int lane = fresh_lane();
    return (lane >> 3) * 4096 + (((lane & 7) ^ ((4 * (w & 1) + (lane >> 4)) & 7)) * 16);
  };
  u64 tag;
  auto get_tag = [&]() {
    const u32x2 t = *(LAS u32x2*)(L + L_CT);
    tag = ((u64)(unsigned)__builtin_amdgcn_readfirstlane(t.y) << 32) | (unsigned)__builtin_amdgcn_readfirstlane(t.x);
  };
  auto await = [&](u32x2& v, int voff) {
    int n = SPIN_CAP;
    asm volatile(
        "1:\n\tv_cmp_ne_u64 vcc, %[tg], %[v]\n\ts_cbranch_vccz 2f\n\ts_sleep 2\n\tbuffer_load_dwordx2 %[v], %[vo], %[rs], 0 offen sc1\n\t"
        "s_sub_u32 %[n], %[n], 1\n\ts_waitcnt vmcnt(0)\n\ts_cbranch_scc0 1b\n2:"
        : [v] "+v"(v), [n] "+s"(n)
        : [tg] "s"(tag), [vo] "v"(voff), [rs] "s"(sWs)
        : "vcc", "scc", "memory");
  };
  u32x4 V[9], Wd[18], cw;
  u32x2 ev;
  unsigned cx;
  const bool mjob = b < 36;
  const u32x4 sC = mksrd(cvec, D * 2), sD = mksrd(mat ? dim : dre, D * D * 2);
  const int wr = (w + 2 * g) & 7;
  unsigned wm;
  __builtin_amdgcn_s_setprio(2);
  if (w >= 4) {
    const int lane = fresh_lane();
    const u32x4 sX = mksrd(x, S * D * 2);
    const int so = (64 * rta + 8 * wr) * 1152;
#pragma unroll
    for (int r = 0; r < 8; ++r) fill<XP>(V[r], lane * 16, sX, so + r * 1152);
    fill<XP>(V[8], (lane >> 3) * 1152 + 1024 + (lane & 7) * 16, sX, so);
  } else {
    const int lane = fresh_lane(), rb = 288 * (b & 1) + 72 * w;
    fill64<1>(ev, 0, sWs, E_OFF);
    asm volatile("buffer_load_dword %0, %1, %2, %3 offen sc1" : "=v"(wm) : "v"(TCH && idx >= 5 && w ? lane * 128 : OOB), "s"(sD), "s"(((idx - 5) * 3 + w - 1) * 8192) : "memory");
    if (mjob) {
      const u32x4 sM = mksrd(wmod, D * 6 * D * 2);
      const int so = rb * 6912 + (b >> 1) * 128;
#pragma unroll
      for (int i = 0; i < 8; ++i) fill<2>(V[i], (lane >> 3) * 8 * 6912 + (lane & 7) * 16, sM, so + i * 6912);
      fill<2>(V[8], (lane >> 3) * 6912 + (lane & 7) * 16, sM, so + 64 * 6912);
      fill<0>(cw, (lane >> 3) * 16, sC, rb * 2);
      asm volatile("buffer_load_ushort %0, %1, %2, %3 offen" : "=v"(cx) : "v"((lane >> 3) * 2), "s"(sC), "s"(rb * 2 + 128) : "memory");
    }
  }
  __builtin_amdgcn_s_setprio(0);
  unsigned ipf;
  {
    u64 pc0;
    unsigned len;
    asm volatile("s_getpc_b64 %0\n.Lfb_lo:\n\ts_mov_b32 %1, .Lfb_hi-.Lfb_lo" : "=s"(pc0), "=s"(len));
    const u32x4 sI = {(unsigned)pc0, (unsigned)(pc0 >> 32) & 0xffffu, len, 0x00020000u};
    asm volatile("buffer_load_dword %0, %1, %2, 0 offen" : "=v"(ipf) : "v"(w == 3 ? fresh_lane() * 128 + (idx & 7) * 8192 : OOB), "s"(sI) : "memory");
  }
  STAMP(6);
  auto wd_dma = [&](int q0, auto NQ) {
    constexpr int nq = NQ;
    const int lane = fresh_lane(), f = 9 * g + (w < 8 ? w : 8);
    const int vo = (8 * (lane >> 5) + ((lane >> 3) & 3)) * 1152 + (((lane & 7) ^ (lane >> 4)) * 16);
    const int so = (32 * (f >> 1) + 4 * (f & 1)) * 1152 + q0 * 128;
#pragma unroll
    for (int i = 0; i < nq; ++i) {
      dma<(WDP == 2)>(lb + w * 10240 + i * 2048, vo, sD, so + i * 128);
      dma<(WDP == 2)>(lb + w * 10240 + i * 2048 + 1024, vo ^ 64, sD, so + i * 128 + 16 * 1152);
    }
  };
  auto wd_get = [&](auto K0, auto N) {
    constexpr int k0 = K0, n = N;
    const int lane = fresh_lane();
    const int p = w * 10240 + (lane & 15) * 128 + (((lane >> 4) ^ ((lane >> 1) & 7)) * 16);
    const int pe = w >= 10 ? p ^ 64 : p, po = w >= 10 ? p + 2048 : p ^ 64;
#pragma unroll
    for (int i = 0; i < n; ++i) Wd[k0 + i] = *(LAS u32x4*)(L + ((i & 1) ? po : pe) + (i >> 1) * 2048);
#pragma unroll
    for (int i = 0; i < n; ++i) PIN(Wd[k0 + i]);
  };
  wd_dma(w >= 10 ? 4 : 0, ic<5>{});
  STAMP(7);
  asm volatile("s_waitcnt vmcnt(10)"
               : "+v"(V[0]), "+v"(V[1]), "+v"(V[2]), "+v"(V[3]), "+v"(V[4]), "+v"(V[5]), "+v"(V[6]), "+v"(V[7]), "+v"(V[8]), "+v"(ev), "+v"(cw),
                 "+v"(cx), "+v"(wm));
  PIN(ipf);
  STAMP(1);
  auto silu = [&](float c) { return rbf(c / (1.f + __expf(-c))); };
  if (w >= 4) {
    const int lane = fresh_lane();
    float s1[8], s2[8], e1, e2, pv = 0.f;
    auto sums = [&](u32x4 v, float p, float& a, float& q) {
      const f32x4 d0 = up4(v.x, v.y) - p, d1 = up4(v.z, v.w) - p, t = d0 + d1, u = d0 * d0 + d1 * d1;
      a = (t[0] + t[1]) + (t[2] + t[3]);
      q = (u[0] + u[1]) + (u[2] + u[3]);
    };
#pragma unroll
    for (int r = 0; r < 8; ++r) {
      const unsigned v0 = V[r].x;
      const float p = bflo(__builtin_amdgcn_readfirstlane(v0));
      sums(V[r], p, s1[r], s2[r]);
      pv = (lane >> 3) == r ? p : pv;
      PIN(V[r]);
      if (r & 1) SCHED();
    }
    sums(V[8], pv, e1, e2);
    PIN(V[8]);
    const float m = rowsums(s1, e1, lane & 8) * (1.f / D), var = rowsums(s2, e2, lane & 8) * (1.f / D) - m * m;
    float rs = rsqrtf(fmaxf(var, 0.f) + 1e-5f), sh = -(pv + m) * rs;
    PIN(rs);
    PIN(sh);
    STAMP(2);
    VMW(0);
    wd_get(ic<0>{}, ic<10>{});
    if (w < 8) {
      wd_dma(5, ic<4>{});
    } else {
      const u32x4 ones = blank();
      const int vl = (CLR & 4) ? fresh_lane() * 16 : OOB;
#pragma unroll
      for (int i = 0; i < 5; ++i) cst128(ones, rWs, w - 8 + 4 * i < 18 ? vl : OOB, abown + (w - 8 + 4 * i) * 1024);
    }
    STAMP(29);
#pragma unroll
    for (int r = 0; r < 9; ++r) {
      const float a = r < 8 ? __uint_as_float(__builtin_amdgcn_readlane(__float_as_uint(rs), 8 * (r & 7))) : rs;
      const float c = r < 8 ? __uint_as_float(__builtin_amdgcn_readlane(__float_as_uint(sh), 8 * (r & 7))) : sh;
      V[r] = pk8(up4(V[r].x, V[r].y) * a + c, up4(V[r].z, V[r].w) * a + c);
      PIN(V[r]);
    }
    if (w < 8) VMW(0);
  } else {
    const int lane = fresh_lane();
    tag = (((u64)(unsigned)__builtin_amdgcn_readfirstlane(ev.y) << 32) | (unsigned)__builtin_amdgcn_readfirstlane(ev.x)) + 1;
    const unsigned tl = (unsigned)tag;
    if (w == 0 && lane == 0) *(LAS u64*)(L + L_CT) = tag;
    bool last = false;
    f32x2 v;
    if (mjob) {
      f32x4 a0 = {0.f, 0.f, 0.f, 0.f}, a1 = a0;
#pragma unroll
      for (int i = 0; i < 9; ++i) {
        const unsigned u = i < 8 ? cw[i >> 1] : cx;
        const float sv = silu((i & 1) ? bfhi(u) : bflo(u));
        a0 += sv * up4(V[i].x, V[i].y);
        a1 += sv * up4(V[i].z, V[i].w);
      }
      *(LAS float*)(L + L_MP + w * 256 + (lane & 7) * 32 + (lane >> 3) * 4) = colsums(a0, a1, lane & 8);
      unsigned n = 0;
      if (lane == 0) n = lds_add(L + C_MP);
      last = __builtin_amdgcn_readfirstlane(n) == 3;
      asm volatile("" ::: "memory");
      LAS char* p = L + L_MP + (lane & 31) * 8;
      v = (*(LAS f32x2*)p + *(LAS f32x2*)(p + 256)) + (*(LAS f32x2*)(p + 512) + *(LAS f32x2*)(p + 768));
    }
#pragma unroll
    for (int i = 0; i < 4; ++i)
      cst128(u32x4{__float_as_uint(v.x), tl, __float_as_uint(v.y), tl}, rWs, last ? (lane >> 5) * 18432 + (lane & 31) * 16 : OOB,
             MG_OFF + i * 36864 + (b & 1) * 9216 + (b >> 1) * 512);
    STAMP(2);
    VMW(4);
    wd_get(ic<0>{}, ic<10>{});
    wd_dma(5, ic<4>{});
    STAMP(29);
    const int mo = MG_OFF + x8 * 18432 + w * 1024, vl = lane * 16, v2 = w == 0 ? vl : OOB;
    u32x4 p[6];
    _Pragma("unroll 1") for (int it = 0; it < SPIN_CAP; ++it) {
#pragma unroll
      for (int i = 0; i < 6; ++i) fill<1>(p[i], i < 4 ? vl : v2, sWs, mo + (i >> 1) * 4096 + (i & 1) * 9216);
      asm volatile("s_waitcnt vmcnt(0)" : "+v"(p[0]), "+v"(p[1]), "+v"(p[2]), "+v"(p[3]), "+v"(p[4]), "+v"(p[5]));
      unsigned d = 0, e = 0;
#pragma unroll
      for (int i = 0; i < 4; ++i) d |= (p[i].y ^ tl) | (p[i].w ^ tl);
#pragma unroll
      for (int i = 4; i < 6; ++i) e |= (p[i].y ^ tl) | (p[i].w ^ tl);
      if (__builtin_amdgcn_ballot_w64((w == 0 ? d | e : d) != 0) == 0) break;
      __builtin_amdgcn_s_sleep(2);
    }
#pragma unroll
    for (int i = 0; i < 3; ++i) {
      unsigned o = pk(__uint_as_float(p[2 * i].x) + __uint_as_float(p[2 * i + 1].x), __uint_as_float(p[2 * i].z) + __uint_as_float(p[2 * i + 1].z));
      if ((w + 4 * i) * 64 + lane >= 288) o = pk(bflo(o) + 1.f, bfhi(o) + 1.f);
      if (i < 2 || w == 0) *(LAS f32x2*)(L + L_SA + (w + 4 * i) * 512 + lane * 8) = f32x2{bflo(o), bfhi(o)};
    }
  }
  STAMP(30);
  u32x4 Wr[6];
  u32x3 cv3;
  const int bn = idx * 12 + w, bph = bn >= 162, bkb = ((bn - 162 * bph) * 57) >> 9, bnb = bn - 162 * bph - 9 * bkb, jm = x8 * 5 + idx - 27;
  {
    const int lane = fresh_lane();
    const int pitch = job ? (bph ? 1152 : F * 2) : 6912, step = job ? 8 * pitch : pitch;
    const int so = job ? (32 * bkb + (bph ? x8 * 576 : 0)) * pitch + (bph ? 0 : x8 * 1152) + bnb * 128 : 48 * w * 6912 + 2304 + jm * 128;
    const u32x4 sW = mksrd(job ? (bph ? w2 : w1) : wmod, job ? D * F * 2 : (jm < 36 ? D * 6 * D * 2 : 0u));
    const int vo = (lane >> 3) * (job ? pitch : 6 * 6912) + (lane & 7) * 16;
#pragma unroll
    for (int i = 0; i < 6; ++i) fill<2>(Wr[i], i < 4 || !job ? vo : OOB, sW, so + i * step);
    asm volatile("buffer_load_dwordx3 %0, %1, %2, %3 offen" : "=v"(cv3) : "v"((lane >> 3) * 12), "s"(sC), "s"(96 * w) : "memory");
  }
  STAMP(31);
  if (w < 8) wd_get(ic<10>{}, ic<8>{});
  BAR();
  STAMP(3);
  u32x2 gv;
  if (w >= 4) {
    get_tag();
    const int lane = fresh_lane();
    LAS char* sp = L + L_SA + lane * 32;
    LAS char* sq = L + L_SA + 2048 + (lane & 7) * 32;
    const f32x4 sa0 = *(LAS f32x4*)sp, sa1 = *(LAS f32x4*)(sp + 16), ca0 = *(LAS f32x4*)(sp + 2304), ca1 = *(LAS f32x4*)(sp + 2320);
    const int hb = L_H + (wr >> 2) * 2048 + (lane & 3) * 256 + (((4 * wr) ^ (lane & 7)) & 15) * 16;
#pragma unroll
    for (int r = 0; r < 8; ++r) {
      *(LAS u32x4*)(L + ((hb + (lane >> 2) * 4096) ^ ((r & 3) * 16)) + (r >> 2) * 1024) = pk8(up4(V[r].x, V[r].y) * ca0 + sa0, up4(V[r].z, V[r].w) * ca1 + sa1);
      if (r & 1) SCHED();
    }
    const f32x4 ta0 = *(LAS f32x4*)sq, ta1 = *(LAS f32x4*)(sq + 16), da0 = *(LAS f32x4*)(sq + 2304), da1 = *(LAS f32x4*)(sq + 2320);
    *(LAS u32x4*)(L + ((hb + 65536 + (lane & 4) * 1024 + (lane >> 5) * 1024) ^ (((lane >> 3) & 3) * 16))) =
        pk8(up4(V[8].x, V[8].y) * da0 + ta0, up4(V[8].z, V[8].w) * da1 + ta1);
    VMW(7);
  } else {
    const int vdma = mk_vdma();
#pragma unroll
    for (int i = 0; i < 16; ++i) ws_dma(ic<C0NT>{}, 0, w + 4 * i, vdma);
  }
  BAR();
  STAMP(4);
  {
    const int lane = fresh_lane();
    const int ra = L_H + (lane >> 4) * 256 + (((lane & 15) ^ (lane >> 4)) * 16);
    const unsigned neg = mat ? 0x80008000u : 0u;
    int fw, fj;
    owner(9 * g + (w < 8 ? w : 8), fw, fj);
    const int so = abown + (fw - 3 * g) * 6144 + fj * 1024;
    f32x4 c[4];
    if (w < 8) {
      if (w == 4 && lane == 0) fst(ws + G0_OFF + ((mat * 4 + g) * 32 + rta) * 8, tag);
      u32x4 Hf[2][4];
#pragma unroll
      for (int rb = 0; rb < 4; ++rb) Hf[0][rb] = *(LAS u32x4*)(L + ra + rb * 1024);
      sfor<18>([&](auto KB) {
        constexpr int kb = KB;
        if (kb < 17) {
#pragma unroll
          for (int rb = 0; rb < 4; ++rb) Hf[(kb + 1) & 1][rb] = *(LAS u32x4*)(L + (ra ^ ((kb + 1) & 1) * 64) + (kb + 1) * 4096 + rb * 1024);
        }
        SCHED();
#pragma unroll
        for (int rb = 0; rb < 4; ++rb) mac(c[rb], Hf[kb & 1][rb], Wd[kb], kb == 0);
        SCHED();
      });
      MFMA_DONE();
      cst128(even8(c[0], c[1]) ^ neg, rWs, lane * 16, so);
      cst128(even8(c[2], c[3]) ^ neg, rWs, lane * 16, so + 3072);
      VMW_SEL(2, 16, 4, );
    } else {
      const int g0 = G0_OFF + ((mat * 4 + g) * 32 + 16 * kh + (lane & 15)) * 8;
      fill64<1>(gv, w == 11 ? g0 : OOB, sWs, 0);
      const int rk = (ra ^ (w >= 10 ? 64 : 0)) + (w >= 10 ? 9 * 4096 : 0) + (w & 1) * 2048;
      u32x4 Hf[4][2];
      auto ldh = [&](int i) {
        Hf[i & 3][0] = *(LAS u32x4*)(L + (rk ^ (i & 1) * 64) + i * 4096);
        Hf[i & 3][1] = *(LAS u32x4*)(L + (rk ^ (i & 1) * 64) + i * 4096 + 1024);
      };
      ldh(0);
      ldh(1);
      ldh(2);
#pragma unroll
      for (int i = 0; i < 9; ++i) {
        if (i < 6) ldh(i + 3);
        SCHED();
        mac(c[0], Hf[i & 3][0], Wd[i], i == 0);
        mac(c[1], Hf[i & 3][1], Wd[i], i == 0);
        SCHED();
      }
      MFMA_DONE();
      LAS char* pp = L + L_PR + (w & 1) * 2048 + lane * 16;
      if (w >= 10) {
        *(LAS f32x4*)pp = c[0];
        *(LAS f32x4*)(pp + 1024) = c[1];
        if (lane == 0) lds_add(L + C_PR + (w & 1) * 4);
        asm volatile("s_waitcnt vmcnt(0)" : "+v"(gv));
        if (w == 11) await(gv, g0);
      } else {
        lds_wait(L + C_PR + (w & 1) * 4, 1);
        cst128(even8(c[0] + *(LAS f32x4*)pp, c[1] + *(LAS f32x4*)(pp + 1024)) ^ neg, rWs, lane * 16, so + (w & 1) * 3072);
        asm volatile("s_waitcnt vmcnt(1)" : "+v"(gv));
      }
    }
  }
#pragma unroll
  for (int i = 0; i < 6; ++i) PIN(Wr[i]);
  PIN(cv3);
  STAMP(5);
  BAR();
  int vl16, rd0;
  {
    const int lane = fresh_lane();
    vl16 = lane * 16;
    rd0 = kq * 16384 + (lane & 15) * 128 + (((lane >> 4) ^ ((lane & 15) >> 1)) * 16);
    LAS char* sp = L + L_H + w * 4096;
    if (job) {
#pragma unroll
      for (int i = 0; i < 4; ++i) *(LAS u32x4*)(sp + i * 1024 + vl16) = Wr[i];
      LAS char* rp = sp + ((lane >> 4) * 8 + ((lane >> 2) & 3)) * 128 + (lane & 3) * 16;
#pragma unroll
      for (int i = 0; i < 4; ++i) {
        const u32x2 lo = __builtin_bit_cast(u32x2, __builtin_amdgcn_ds_read_tr16_b64_v4i16((LAS s16x4*)(rp + (i >> 1) * 64 + (i & 1) * 8)));
        const u32x2 hi = __builtin_bit_cast(u32x2, __builtin_amdgcn_ds_read_tr16_b64_v4i16((LAS s16x4*)(rp + (i >> 1) * 64 + (i & 1) * 8 + 512)));
        Wr[i] = u32x4{lo.x, lo.y, hi.x, hi.y};
      }
    } else {
      f32x4 a0 = {0.f, 0.f, 0.f, 0.f}, a1 = a0;
#pragma unroll
      for (int i = 0; i < 6; ++i) {
        const unsigned u = cv3[i >> 1];
        const float sv = silu((i & 1) ? bfhi(u) : bflo(u));
        a0 += sv * up4(Wr[i].x, Wr[i].y);
        a1 += sv * up4(Wr[i].z, Wr[i].w);
      }
      *(LAS float*)(sp + (lane & 7) * 32 + (lane >> 3) * 4) = colsums(a0, a1, lane & 8);
      unsigned n = 0;
      if (lane == 0) n = lds_add(L + C_MV);
      const bool last = __builtin_amdgcn_readfirstlane(n) == 11;
      asm volatile("" ::: "memory");
      f32x4 v = {0.f, 0.f, 0.f, 0.f};
#pragma unroll
      for (int k = 0; k < 12; ++k) v += *(LAS f32x4*)(L + L_H + k * 4096 + (lane & 15) * 16);
      u32x2 o = pk4(v);
      if (jm >= 18 && jm < 27) o = pk4(up4(o.x, o.y) + 1.f);
      cst128(u32x4{o.x, (unsigned)tag, o.y, (unsigned)tag}, rWs, last && jm < 36 && lane < 16 ? vl16 : OOB, MV_OFF + jm * 256);
    }
  }
  const int abin = AB_OFF + ((mat * 4 + g) * 32 + 16 * kh + kq) * 18432 + cg * 6144;
  auto ab_at = [&](int c) { return abin + ((c + crot) & 3) * 73728; };
  u32x4 Ra[6];
  _Pragma("unroll 1") for (int it = 0; it < SPIN_CAP; ++it) {
#pragma unroll
    for (int i = 0; i < 6; ++i) fill<1>(Ra[i], vl16, sWs, ab_at(0) + i * 1024);
    asm volatile("s_waitcnt vmcnt(0)" : "+v"(Ra[0]), "+v"(Ra[1]), "+v"(Ra[2]), "+v"(Ra[3]), "+v"(Ra[4]), "+v"(Ra[5]));
    unsigned m = 0;
#pragma unroll
    for (int i = 0; i < 6; ++i) m = top(m, Ra[i]);
    if (!absent(m)) break;
  }
  STAMP(8);
#pragma unroll
  for (int i = 0; i < 4; ++i) {
    int fw, fj;
    owner(4 * bnb + i, fw, fj);
    cst128(Wr[i], rWs, job ? vl16 : OOB, WT_OFF + (x8 * 12 + fw) * 110592 + (bph * 54 + bkb * 3 + fj) * 1024);
  }
  BAR();
  STAMP(9);
  f32x4 acc[8][3];
  auto touch = [&](unsigned& r, int ph) {
    const int lane = fresh_lane();
    asm volatile("buffer_load_dword %0, %1, %2, %3 offen sc1"
                 : "=v"(r)
                 : "v"(lane < 27 && (T0 || ph) ? lane * 128 : OOB), "s"(sWs), "s"(WT_OFF + (sl * 12 + w) * 110592 + ph * 55296 + si * 3456)
                 : "memory");
  };
  {
    u32x4 Wf[2][4];
    auto ldw = [&](int s, int h) {
#pragma unroll
      for (int r = 0; r < 4; ++r) Wf[h][r] = *(LAS u32x4*)(L + ((s >> 1) & 1) * 65536 + (4 * h + r) * 2048 + (rd0 ^ ((s & 1) * 64)));
    };
    auto gw_at = [&]() { return GW_OFF + (sl * 32 + (fresh_lane() & 31)) * 8; };
    const int wq = w < 9 ? w : w - 9;
    sfor<8>([&](auto SS) {
      constexpr int s = SS, c = s >> 1, kk = s & 1;
      if constexpr (s >= 2) {
        constexpr int y = s == 7 ? 2 : (kk ? (s == 3 ? 11 : 9) : 3);
        asm volatile("s_waitcnt vmcnt(%3)" : "+v"(Ra[3 * kk]), "+v"(Ra[3 * kk + 1]), "+v"(Ra[3 * kk + 2]) : "i"(y));
      }
      int vdma;
      if constexpr (kk == 0) {
        if constexpr (s > 0) BAR();
        STAMP(10 + c);
        if constexpr (s == 4) {
          u32x4 mv = *(LAS u32x4*)(L + L_X2 + wq * 1024 + vl16);
          ldw(s, 0);
          _Pragma("unroll 1") for (int it = 0; it < SPIN_CAP; ++it) {
            if (__builtin_amdgcn_ballot_w64(((mv.y ^ (unsigned)tag) | (mv.w ^ (unsigned)tag)) != 0) == 0) break;
            refill(mv, vl16, sWs, MV_OFF + wq * 1024);
            asm volatile("s_waitcnt vmcnt(0)" : "+v"(mv));
          }
          *(LAS f32x4*)(L + L_MV + wq * 1024 + vl16) = up4(mv.x, mv.z);
          SCHED();
        } else {
          ldw(s, 0);
        }
        ldw(s, 1);
        SCHED();
        if constexpr (c < 3) vdma = mk_vdma();
        if constexpr (s == 2) {
          if (w == 11 && fresh_lane() == 0) fst(ws + GW_OFF + (x8 * 32 + idx) * 8, tag);
          dma<2>(lb + L_X2 + wq * 1024, vl16, sWs, MV_OFF + wq * 1024);
        }
        if constexpr (s == 4) {
          if (w == 11 && fresh_lane() == 0) fst(ws + G1_OFF + pc * 8, tag);
          fill64<1>(gv, gw_at(), sWs, 0);
        }
        if constexpr (s == 6) {
          PIN(gv);
          await(gv, gw_at());
          touch(wm, 0);
          fill64<1>(gv, G1_OFF + (16 * R + (fresh_lane() & 15)) * 8, sWs, 0);
        }
      }
      if constexpr (s >= 2) {
        if (absent(top(top(top(0u, Ra[3 * kk]), Ra[3 * kk + 1]), Ra[3 * kk + 2]))) {
          _Pragma("unroll 1") for (int it = 0; it < SPIN_CAP; ++it) {
#pragma unroll
            for (int j = 0; j < 3; ++j) refill(Ra[3 * kk + j], vl16, sWs, ab_at(c) + kk * 3072 + j * 1024);
            asm volatile("s_waitcnt vmcnt(0)" : "+v"(Ra[3 * kk]), "+v"(Ra[3 * kk + 1]), "+v"(Ra[3 * kk + 2]));
            if (!absent(top(top(top(0u, Ra[3 * kk]), Ra[3 * kk + 1]), Ra[3 * kk + 2]))) break;
          }
        }
      }
      SCHED();
      sfor<3>([&](auto J) {
        constexpr int j = J;
#pragma unroll
        for (int r = 0; r < 4; ++r) mac(acc[r][j], Ra[3 * kk + j], Wf[0][r], s == 0);
        SCHED();
        if constexpr (kk == 0 && c < 3) ws_dma(ic<0>{}, c + 1, w + 12 * (2 * j), vdma);
        if constexpr (kk == 0 && s < 4 && j < 2)
          cst128(blank(), rWs, (CLR & 1) && (s < 2 || j < 1 || w < 9) ? vl16 : OOB, XB_OFF + pc * 46080 + (w + 12 * (s + j)) * 1024);
        if constexpr (kk == 0 && j == 2) ldw(s + 1, 0);
        SCHED();
#pragma unroll
        for (int r = 0; r < 4; ++r) mac(acc[4 + r][j], Ra[3 * kk + j], Wf[1][r], s == 0);
        SCHED();
        if constexpr (kk == 0 && j == 2) ldw(s + 1, 1);
        if constexpr (c < 3) refill(Ra[3 * kk + j], vl16, sWs, ab_at(c + 1) + kk * 3072 + j * 1024);
        if constexpr (kk == 0 && c < 3) {
          if constexpr (j < 2) ws_dma(ic<0>{}, c + 1, w + 12 * (2 * j + 1), vdma);
          else if (w < 4) ws_dma(ic<0>{}, c + 1, 60 + w, vdma);
        }
        SCHED();
      });
    });
  }
  MFMA_DONE();
  __builtin_amdgcn_s_setprio(1);
  STAMP(14);
  auto fin_map = [&](int lane, int& flo, int& fhi, bool& alo, bool& ahi) {
    const int s = lane >> 4, f2 = 3 * w + ((w & 1) ? 0 : 2);
    flo = 8 * ((lane >> 2) & 3) + (s < 2 ? 32 * ((3 * w + 1) >> 1) : 32 * (f2 >> 1) + 4 * (f2 & 1));
    fhi = flo + (s < 2 ? 4 : 0);
    alo = s == 1;
    ahi = s >= 1;
  };
  const int row0 = 32 * (pc >> 2) + 4 * (pc & 3);
  const int rot = ROT ? (si * 18) >> 4 : 0, wb = WT_OFF + (sl * 12 + w) * 110592;
  auto w_at = [&](int it) {
    const int ph = it / 54, k = (it - 54 * ph) / 3 + rot;
    return wb + ph * 55296 + (k >= 18 ? k - 18 : k) * 3072 + (it % 3) * 1024;
  };
  u32x4 Rw[18];
  {
    auto area = [&](int v) { return v < 7 ? v * 9216 : (v == 7 ? 131072 : 65536 + (v - 8) * 16384); };
    u32x4 t[9], plo, phi;
#pragma unroll
    for (int i = 1; i < 4; ++i) {
      t[3 * i - 3] = pk8(acc[2 * i][0], acc[2 * i][1]);
      t[3 * i - 2] = pk8(acc[2 * i + 1][0], acc[2 * i + 1][1]);
      t[3 * i - 1] = pk8(acc[2 * i][2], acc[2 * i + 1][2]);
    }
    if (w >= 8) lds_wait(L + C_Q + kq * 4, 2);
    else if (vl16 == 0) lds_add(L + C_Q + kq * 4);
#pragma unroll
    for (int i = 0; i < 9; ++i) *(LAS u32x4*)(L + area(w) + i * 1024 + vl16) = t[i];
    if (vl16 == 0) lds_add(L + C_R + cg * 4);
    const int lane = fresh_lane(), v48 = lane < 48 ? vl16 : OOB;
    int flo, fhi;
    bool alo, ahi;
    fin_map(lane, flo, fhi, alo, ahi);
    u32x2 xa, xb;
    {
      const u32x4 sX = mksrd(x, S * D * 2);
      fill64<0>(xa, lane < 48 ? (16 * alo + (lane & 3)) * 1152 + flo * 2 : OOB, sX, row0 * 1152);
      fill64<0>(xb, lane < 48 ? (16 * ahi + (lane & 3)) * 1152 + fhi * 2 : OOB, sX, row0 * 1152);
    }
    pickers(plo, phi);
    lds_wait(L + C_R + cg * 4, 4);
    STAMP(15);
#pragma unroll
    for (int i = 0; i < 9; ++i) t[i] = *(LAS u32x4*)(L + area(w ^ (i / 3 + 1)) + i * 1024 + vl16);
#pragma unroll
    for (int i = 0; i < 3; ++i) {
      mac(acc[0][0], plo, t[3 * i], false);
      mac(acc[0][1], phi, t[3 * i], false);
      mac(acc[1][0], plo, t[3 * i + 1], false);
      mac(acc[1][1], phi, t[3 * i + 1], false);
      mac(acc[0][2], plo, t[3 * i + 2], false);
      mac(acc[1][2], phi, t[3 * i + 2], false);
    }
    MFMA_DONE();
    asm volatile("s_waitcnt vmcnt(2)" : "+v"(gv), "+v"(wm));
    await(gv, G1_OFF + (16 * R + (lane & 15)) * 8);
    const int vpub = ((lane >> 2) & 3) * 46080 + (lane >> 4) * 64 + (lane & 3) * 16;
    const int yb = XB_OFF + (4 * R + kq) * 4 * 46080 + kq4 * 9216 + (3 * g + cg) * 768;
    cst128(even8(acc[0][0], acc[0][1]), rWs, vpub, yb);
    cst128(even8(acc[1][0], acc[1][1]), rWs, vpub, yb + 256);
    cst128(even8(acc[0][2], acc[1][2]), rWs, vpub, yb + 512);
    STAMP(16);
    u32x4 yp[4];
    __builtin_amdgcn_s_setprio(0);
#pragma unroll
    for (int i = 0; i < 9; ++i) fill<1>(Rw[i], vl16, sWs, w_at(i));
#pragma unroll
    for (int p = 0; p < 4; ++p) fill<1>(yp[p], v48, sWs, XB_OFF + pc * 46080 + p * 9216 + w * 768);
    __builtin_amdgcn_s_setprio(1);
    STAMP(17);
    asm volatile("s_waitcnt vmcnt(0)" : "+v"(yp[0]), "+v"(yp[1]), "+v"(yp[2]), "+v"(yp[3]), "+v"(xa), "+v"(xb), "+v"(wm));
    _Pragma("unroll 1") for (int it = 0; it < SPIN_CAP; ++it) {
      if (!absent(top(top(top(top(0u, yp[0]), yp[1]), yp[2]), yp[3]))) break;
      __builtin_amdgcn_s_sleep(1);
#pragma unroll
      for (int p = 0; p < 4; ++p) refill(yp[p], v48, sWs, XB_OFF + pc * 46080 + p * 9216 + w * 768);
      asm volatile("s_waitcnt vmcnt(0)" : "+v"(yp[0]), "+v"(yp[1]), "+v"(yp[2]), "+v"(yp[3]));
    }
    STAMP(18);
    f32x4 yl, yh;
    SCHED();
#pragma unroll
    for (int p = 0; p < 4; ++p) {
      mac(yl, plo, yp[p], p == 0);
      mac(yh, phi, yp[p], p == 0);
    }
    SCHED();
    const f32x4 gal = *(LAS f32x4*)(L + L_MV + flo * 4), gah = *(LAS f32x4*)(L + L_MV + fhi * 4);
    const f32x4 sbl = *(LAS f32x4*)(L + L_MV + 2304 + flo * 4), sbh = *(LAS f32x4*)(L + L_MV + 2304 + fhi * 4);
    const f32x4 cbl = *(LAS f32x4*)(L + L_MV + 4608 + flo * 4), cbh = *(LAS f32x4*)(L + L_MV + 4608 + fhi * 4);
    MFMA_DONE();
    const u32x4 x2 = pk8(up4(xa.x, xa.y) + gal * yl, up4(xb.x, xb.y) + gah * yh);
    yl = up4(x2.x, x2.y);
    yh = up4(x2.z, x2.w);
    {
      const bool on = lane < 48;
      const float lo = (yl[0] + yl[1]) + (yl[2] + yl[3]), hi = (yh[0] + yh[1]) + (yh[2] + yh[3]);
      const float ma = sum16((alo ? 0.f : lo) + (ahi ? 0.f : hi)) * (1.f / 48), mb = sum16((alo ? lo : 0.f) + (ahi ? hi : 0.f)) * (1.f / 48);
      const f32x4 dl = yl - (alo ? mb : ma), dh = yh - (ahi ? mb : ma), ql = dl * dl, qh = dh * dh;
      const float q0 = on ? (ql[0] + ql[1]) + (ql[2] + ql[3]) : 0.f, q1 = on ? (qh[0] + qh[1]) + (qh[2] + qh[3]) : 0.f;
      const float qa = sum16((alo ? 0.f : q0) + (ahi ? 0.f : q1)), qb = sum16((alo ? q0 : 0.f) + (ahi ? q1 : 0.f));
      if (lane < 4) *(LAS f32x4*)(L + L_ST + (w * 4 + lane) * 16) = f32x4{ma, mb, qa, qb};
    }
    if (lane < 48) *(LAS u32x4*)(L + L_X2 + w * 768 + vl16) = x2;
    BAR();
    STAMP(19);
    {
      const f32x4 e = *(LAS f32x4*)(L + L_ST + (lane < 48 ? vl16 : 0));
      const bool on = lane < 48;
      const float ma = sum16(on ? e[0] : 0.f) * (1.f / 12), mb = sum16(on ? e[1] : 0.f) * (1.f / 12);
      const float da = e[0] - ma, db = e[1] - mb;
      const float va = rsqrtf(sum16(on ? e[2] + 48.f * da * da : 0.f) * (1.f / D) + 1e-5f);
      const float vb = rsqrtf(sum16(on ? e[3] + 48.f * db * db : 0.f) * (1.f / D) + 1e-5f);
      cst128(even8((yl - (alo ? mb : ma)) * (alo ? vb : va) * cbl + sbl, (yh - (ahi ? mb : ma)) * (ahi ? vb : va) * cbh + sbh), rWs, v48,
             XB_OFF + pc * 46080 + 36864 + w * 768);
    }
    if (w == 11 && b == 0 && lane == 0) fst(ws + E_OFF, tag);
    u32x4 h[8];
    __builtin_amdgcn_s_setprio(0);
#pragma unroll
    for (int i = 9; i < 18; ++i) fill<1>(Rw[i], vl16, sWs, w_at(i));
#pragma unroll
    for (int p = 0; p < 8; ++p) fill<1>(h[p], v48, sWs, XB_OFF + (rt * 8 + p) * 46080 + 36864 + w * 768);
    STAMP(20);
    const int kp = (flo >> 5) - rot, lg = (lane >> 2) & 3;
    const int hl = (kp < 0 ? kp + 18 : kp) * 4096 + lg * 256 + (lg & 1) * 192 + (lane & 3) * 16 + (flo & 4) * 2;
    const int olo = hl + alo * 1024, ohi = hl + ahi * 1024 + (fhi - flo) * 2;
    asm volatile("s_waitcnt vmcnt(0)" : "+v"(h[0]), "+v"(h[1]), "+v"(h[2]), "+v"(h[3]), "+v"(h[4]), "+v"(h[5]), "+v"(h[6]), "+v"(h[7]));
    _Pragma("unroll 1") for (int it = 0; it < SPIN_CAP; ++it) {
      unsigned m = 0;
#pragma unroll
      for (int p = 0; p < 8; ++p) m = top(m, h[p]);
      if (!absent(m)) break;
      __builtin_amdgcn_s_sleep(1);
#pragma unroll
      for (int p = 0; p < 8; ++p) refill(h[p], v48, sWs, XB_OFF + (rt * 8 + p) * 46080 + 36864 + w * 768);
      asm volatile("s_waitcnt vmcnt(0)" : "+v"(h[0]), "+v"(h[1]), "+v"(h[2]), "+v"(h[3]), "+v"(h[4]), "+v"(h[5]), "+v"(h[6]), "+v"(h[7]));
    }
    STAMP(21);
    if (lane < 48) {
#pragma unroll
      for (int p = 0; p < 8; ++p) {
        *(LAS u32x2*)(L + (olo ^ ((p & 3) * 64)) + (p >> 2) * 2048) = u32x2{h[p].x, h[p].y};
        *(LAS u32x2*)(L + (ohi ^ ((p & 3) * 64)) + (p >> 2) * 2048) = u32x2{h[p].z, h[p].w};
      }
    }
  }
#pragma unroll
  for (int i = 0; i < 18; ++i) PIN(Rw[i]);
  BAR();
  if (w == 11 && vl16 == 0) __hip_atomic_store((LAG unsigned*)(ws + G3_OFF + pc * 4), (unsigned)tag, __ATOMIC_RELAXED, __HIP_MEMORY_SCOPE_AGENT);
  STAMP(22);
  f32x4 am[4][3];
  u32x4 g3;
  {
    sfor<2>([&](auto PH) {
      constexpr int ph = PH;
      LAS char* A = L + ph * L_H1 + (vl16 ^ ((fresh_lane() & 16) * 12));
      u32x4 Bf[2][4];
#pragma unroll
      for (int mb = 0; mb < 4; ++mb) Bf[0][mb] = *(LAS u32x4*)(A + mb * 1024);
      sfor<18>([&](auto KB) {
        constexpr int kb = KB;
        if (kb < 17) {
#pragma unroll
          for (int mb = 0; mb < 4; ++mb) Bf[(kb + 1) & 1][mb] = *(LAS u32x4*)(A + ((kb + 1) * 4 + mb) * 1024);
        }
        SCHED();
        sfor<3>([&](auto J) {
          constexpr int j = J, it = ph * 54 + kb * 3 + j;
          if constexpr (it >= 18) asm volatile("s_waitcnt vmcnt(%1)" : "+v"(Rw[it % 18]) : "i"(younger(it)));
#pragma unroll
          for (int mb = 0; mb < 4; ++mb) mac(am[mb][j], Rw[it % 18], Bf[kb & 1][mb], kb == 0);
          SCHED();
          constexpr int j0 = issued(it), j1 = issued(it + 1);
          sfor<j1 - j0>([&](auto JJ) {
            constexpr int jj = j0 + JJ;
            static_assert(jj <= it, "slot still live");
            refill(Rw[jj % 18], vl16, sWs, w_at(jj + 18));
          });
          if constexpr (it == X_W2) touch(wm, 1);
          if constexpr (it == 53) PIN(wm);
          if constexpr (it == X_OP) fill<1>(g3, vl16, sWs, G3_OFF);
          SCHED();
        });
      });
      MFMA_DONE();
      STAMP(23 + 2 * ph);
      if constexpr (ph == 0) {
        const int fp = ((3 * w + 1) >> 1) - rot, fh = ((3 * (w & ~1) + 2) >> 1) - rot;
        LAS char* Hp = A + L_H1 + (fp < 0 ? fp + 18 : fp) * 4096;
        LAS char* Hh = A + L_H1 + (fh < 0 ? fh + 18 : fh) * 4096 + (w & 1) * 8;
#pragma unroll
        for (int mb = 0; mb < 4; ++mb) {
          *(LAS u32x4*)(Hp + mb * 1024) = pk8(relu4(am[mb][0]), relu4(am[mb][1]));
          *(LAS u32x2*)(Hh + mb * 1024) = pk4(relu4(am[mb][2]));
        }
        BAR();
        STAMP(24);
      }
    });
  }
  {
    const int lane = fresh_lane(), v48 = lane < 48 ? vl16 : OOB;
    auto inbox = [&](int p, int n) { return n < 5 ? XB_OFF + p * 46080 + n * 9216 : AB_OFF + p * 18432 + (n - 5) * 9216; };
    PIN(g3);
    _Pragma("unroll 1") for (int it = 0; it < SPIN_CAP; ++it) {
      const unsigned tl = (unsigned)tag;
      if (__builtin_amdgcn_ballot_w64((((g3.x ^ tl) | (g3.y ^ tl)) | ((g3.z ^ tl) | (g3.w ^ tl))) != 0) == 0) break;
      __builtin_amdgcn_s_sleep(2);
      refill(g3, vl16, sWs, G3_OFF);
      asm volatile("s_waitcnt vmcnt(0)" : "+v"(g3));
    }
    const int vq = (lane >> 4) * 64 + (lane & 3) * 16;
    LAS char* keep = L + w * 768;
#pragma unroll
    for (int h = 0; h < 2; ++h) {
      const int p = 4 * h + ((lane >> 2) & 3), n = (sl - p - 1) & 7, vo = n < 7 ? inbox(rt * 8 + p, n) + vq : OOB;
      const u32x4 o0 = odd8(am[2 * h][0], am[2 * h][1]), o1 = odd8(am[2 * h + 1][0], am[2 * h + 1][1]), o2 = odd8(am[2 * h][2], am[2 * h + 1][2]);
      cst128(o0, rWs, vo, w * 768);
      cst128(o1, rWs, vo, w * 768 + 256);
      cst128(o2, rWs, vo, w * 768 + 512);
      if (n == 7) {
        *(LAS u32x4*)(keep + vq) = o0;
        *(LAS u32x4*)(keep + vq + 256) = o1;
        *(LAS u32x4*)(keep + vq + 512) = o2;
      }
    }
    STAMP(26);
    BAR();
    int flo, fhi;
    bool alo, ahi;
    fin_map(lane, flo, fhi, alo, ahi);
    const u32x4 x2 = *(LAS u32x4*)(L + L_X2 + w * 768 + vl16);
    const f32x4 gl = *(LAS f32x4*)(L + L_GB + flo * 4), gh = *(LAS f32x4*)(L + L_GB + fhi * 4);
    u32x4 p[8], plo, phi;
    p[7] = *(LAS u32x4*)(keep + (lane < 48 ? vl16 : 0));
    pickers(plo, phi);
    _Pragma("unroll 1") for (int it = 0; it < SPIN_CAP; ++it) {
#pragma unroll
      for (int k = 0; k < 7; ++k) fill<1>(p[k], v48, sWs, inbox(pc, k) + w * 768);
      asm volatile("s_waitcnt vmcnt(0)" : "+v"(p[0]), "+v"(p[1]), "+v"(p[2]), "+v"(p[3]), "+v"(p[4]), "+v"(p[5]), "+v"(p[6]));
      u32x4 m = p[0];
#pragma unroll
      for (int k = 1; k < 7; ++k) m &= p[k];
      if (__builtin_amdgcn_ballot_w64(lane < 48 && (m.x & m.y & m.z & m.w & 1u) == 0) == 0) break;
      __builtin_amdgcn_s_sleep(2);
    }
    STAMP(27);
    f32x4 sl4, sh4, sl5, sh5;
    SCHED();
#pragma unroll
    for (int k = 0; k < 8; k += 2) {
      mac(sl4, plo, p[k], k == 0);
      mac(sh4, phi, p[k], k == 0);
      mac(sl5, plo, p[k + 1], k == 0);
      mac(sh5, phi, p[k + 1], k == 0);
    }
    MFMA_DONE();
    sl4 += sl5;
    sh4 += sh5;
    const rsrc_t rO = mkrs(out, S * D * 2);
    const int olo = lane < 48 ? (16 * alo + (lane & 3)) * 1152 + flo * 2 : OOB, ohi = lane < 48 ? (16 * ahi + (lane & 3)) * 1152 + fhi * 2 : OOB;
    __builtin_amdgcn_raw_buffer_store_b64(pk4(up4(x2.x, x2.y) + gl * sl4), rO, olo, row0 * 1152, 0);
    __builtin_amdgcn_raw_buffer_store_b64(pk4(up4(x2.z, x2.w) + gh * sh4), rO, ohi, row0 * 1152, 0);
  }
  asm volatile(".Lfb_hi:");
  STAMP(28);
}

extern "C" void kernel_launch(void* const* d_in, const int* in_sizes, int n_in, void* d_out, int out_size, void* d_ws, size_t ws_size,
                              hipStream_t stream) {
  (void)hipFuncSetAttribute((const void*)fused_block, hipFuncAttributeMaxDynamicSharedMemorySize, LDS_BYTES);
  fused_block<<<256, 768, LDS_BYTES, stream>>>((const u16*)d_in[0], (const u16*)d_in[1], (const u16*)d_in[2], (const u16*)d_in[3],
                                               (const u16*)d_in[4], (const u16*)d_in[6], (const u16*)d_in[7], (const u16*)d_in[8],
                                               (const u16*)d_in[9], (char*)d_ws, (u16*)d_out);
}
```

```cpp
#include <hip/hip_runtime.h>
#include <hip/hip_bf16.h>
#include <stdint.h>
#include <utility>

typedef unsigned short u16;
typedef unsigned long long u64;
typedef __attribute__((ext_vector_type(2))) float f32x2;
typedef __attribute__((ext_vector_type(4))) float f32x4;
typedef __attribute__((ext_vector_type(2))) __bf16 bf16x2;
typedef __attribute__((ext_vector_type(4))) short s16x4;
typedef __attribute__((ext_vector_type(2))) unsigned u32x2;
typedef __attribute__((ext_vector_type(3))) unsigned u32x3;
typedef __attribute__((ext_vector_type(4))) unsigned u32x4;
typedef __amdgpu_buffer_rsrc_t rsrc_t;
#define LAS __attribute__((address_space(3)))
#define LAG __attribute__((address_space(1)))
typedef LAG u64 gu64;

constexpr int S = 2048, D = 576, F = 4608;
#define WDP 0
#define ROT 1
#define CLR 7
#define EXP2 0
#define XP 1
#define CROT 1
#define C0NT 0
#define TCH 1
#define IPF 1
#define T0 1
#define PL 0
#define P1 6
#define P2 11
#define P3 15
#define Q1 4
#define Q2 8
#define Q3 12
#define OST 0
#define DUE 95
#define FLIP 1
constexpr unsigned E_OFF = 0, G0_OFF = 0x1000, G1_OFF = 0x2000, GW_OFF = 0x3000, G3_OFF = 0x5000;
constexpr unsigned MG_OFF = 0x8000;
constexpr unsigned MV_OFF = 0x2c000;
constexpr unsigned AB_OFF = 0x30000;
constexpr unsigned XB_OFF = AB_OFF + 256 * 18432;
constexpr unsigned WT_OFF = XB_OFF + 256 * 46080;
constexpr unsigned WS_END = WT_OFF + 96 * 110592;
constexpr int SC1 = 16;
constexpr int OOB = (int)0x80000000u;
constexpr int L_H = 65536, L_SA = 139264, L_MP = 143872, L_PR = 144384;
constexpr int L_H1 = 73728, L_MV = 140544, L_GB = 147456, L_X2 = 149760, L_CT = 158976, L_STAMP = 159232, L_ST = 162304;
constexpr int C_MP = L_CT + 16, C_PR = L_CT + 20, C_MV = L_CT + 28, C_Q = L_CT + 32, C_R = L_CT + 48;
constexpr int LDS_BYTES = 163840;
constexpr int SPIN_CAP = 1 << 16;

template <int N>
using ic = std::integral_constant<int, N>;
template <class Fn, int... I>
__device__ __forceinline__ void sfor_(Fn&& f, std::integer_sequence<int, I...>) { (f(std::integral_constant<int, I>{}), ...); }
template <int N, class Fn>
__device__ __forceinline__ void sfor(Fn&& f) { sfor_(f, std::make_integer_sequence<int, N>{}); }

__device__ __forceinline__ rsrc_t mkrs(const void* p, unsigned bytes) { return __builtin_amdgcn_make_buffer_rsrc((void*)p, 0, bytes, 0x00020000); }
__device__ __forceinline__ u32x4 mksrd(const void* p, unsigned bytes) {
  const u64 a = (u64)p;
  return u32x4{(unsigned)a, (unsigned)(a >> 32) & 0xffffu, bytes, 0x00020000u};
}
#define SCHED() __builtin_amdgcn_sched_barrier(0)
__device__ __forceinline__ void cst128(u32x4 d, rsrc_t r, int v, int s) {
  __builtin_amdgcn_raw_buffer_store_b128(d, r, v, s, SC1);
  SCHED();
  asm volatile("s_nop 1" ::: "memory");
  SCHED();
}
template <int P>
__device__ __forceinline__ void dma(unsigned lds, int voff, u32x4 srd, int soff) {
  if (P == 2) asm volatile("s_mov_b32 m0, %0\n\ts_nop 3\n\tbuffer_load_dwordx4 %1, %2, %3 offen sc1 lds" ::"s"(lds), "v"(voff), "s"(srd), "s"(soff) : "memory");
  else if (P == 1) asm volatile("s_mov_b32 m0, %0\n\ts_nop 3\n\tbuffer_load_dwordx4 %1, %2, %3 offen nt lds" ::"s"(lds), "v"(voff), "s"(srd), "s"(soff) : "memory");
  else asm volatile("s_mov_b32 m0, %0\n\ts_nop 3\n\tbuffer_load_dwordx4 %1, %2, %3 offen lds" ::"s"(lds), "v"(voff), "s"(srd), "s"(soff) : "memory");
}
template <int P>
__device__ __forceinline__ void fill(u32x4& r, int voff, u32x4 srd, int soff) {
  if (P == 1) asm volatile("buffer_load_dwordx4 %0, %1, %2, %3 offen sc1" : "=v"(r) : "v"(voff), "s"(srd), "s"(soff) : "memory");
  else if (P == 2) asm volatile("buffer_load_dwordx4 %0, %1, %2, %3 offen nt" : "=v"(r) : "v"(voff), "s"(srd), "s"(soff) : "memory");
  else asm volatile("buffer_load_dwordx4 %0, %1, %2, %3 offen" : "=v"(r) : "v"(voff), "s"(srd), "s"(soff) : "memory");
}
__device__ __forceinline__ void refill(u32x4& r, int voff, u32x4 srd, int soff) {
  asm volatile("buffer_load_dwordx4 %0, %1, %2, %3 offen sc1" : "+v"(r) : "v"(voff), "s"(srd), "s"(soff) : "memory");
}
template <int P>
__device__ __forceinline__ void fill64(u32x2& r, int voff, u32x4 srd, int soff) {
  if (P == 1) asm volatile("buffer_load_dwordx2 %0, %1, %2, %3 offen sc1" : "=v"(r) : "v"(voff), "s"(srd), "s"(soff) : "memory");
  else asm volatile("buffer_load_dwordx2 %0, %1, %2, %3 offen" : "=v"(r) : "v"(voff), "s"(srd), "s"(soff) : "memory");
}
#define BAR() asm volatile("s_waitcnt lgkmcnt(0)\n\ts_barrier" ::: "memory")
#define VMW(n) asm volatile("s_waitcnt vmcnt(%0)" ::"i"(n) : "memory")
#define VMW_SEL(n, d, lim, ...) \
  asm volatile("s_waitcnt vmcnt(%[hi])\n\ts_cmp_lt_u32 %[ww], %[li]\n\ts_cbranch_scc1 1f\n\ts_waitcnt vmcnt(%[lo])\n1:" \
               : __VA_ARGS__ : [lo] "i"(n), [hi] "i"((n) + (d)), [li] "i"(lim), [ww] "s"(w) : "scc", "memory")
#define PIN(x) asm volatile("" : "+v"(x))
__device__ __forceinline__ int fresh_lane() {
  int l;
  asm volatile("v_mbcnt_lo_u32_b32 %0, -1, 0\n\tv_mbcnt_hi_u32_b32 %0, -1, %0" : "=v"(l));
  return l;
}
__device__ __forceinline__ float bflo(unsigned u) { return __uint_as_float(u << 16); }
__device__ __forceinline__ float bfhi(unsigned u) { return __uint_as_float(u & 0xffff0000u); }
__device__ __forceinline__ unsigned pk(float a, float b) {
  f32x2 v = {a, b};
  return __builtin_bit_cast(unsigned, __builtin_convertvector(v, bf16x2));
}
__device__ __forceinline__ float rbf(float a) { return bflo(pk(a, 0.f)); }
__device__ __forceinline__ float dot2(unsigned a, unsigned b, float c) {
  return __builtin_amdgcn_fdot2_f32_bf16(__builtin_bit_cast(bf16x2, a), __builtin_bit_cast(bf16x2, b), c, false);
}
__device__ __forceinline__ void axpy8(f32x4& a0, f32x4& a1, u32x4 v, unsigned lo, unsigned hi) {
  a0 = f32x4{dot2(v.x, lo, a0[0]), dot2(v.x, hi, a0[1]), dot2(v.y, lo, a0[2]), dot2(v.y, hi, a0[3])};
  a1 = f32x4{dot2(v.z, lo, a1[0]), dot2(v.z, hi, a1[1]), dot2(v.w, lo, a1[2]), dot2(v.w, hi, a1[3])};
}
__device__ __forceinline__ u32x2 pk4(f32x4 a) { return u32x2{pk(a[0], a[1]), pk(a[2], a[3])}; }
__device__ __forceinline__ u32x4 pk8(f32x4 a, f32x4 b) { return u32x4{pk(a[0], a[1]), pk(a[2], a[3]), pk(b[0], b[1]), pk(b[2], b[3])}; }
__device__ __forceinline__ f32x4 up4(unsigned a, unsigned b) { return f32x4{bflo(a), bfhi(a), bflo(b), bfhi(b)}; }
__device__ __forceinline__ f32x4 relu4(f32x4 a) { return f32x4{fmaxf(a[0], 0.f), fmaxf(a[1], 0.f), fmaxf(a[2], 0.f), fmaxf(a[3], 0.f)}; }
__device__ __forceinline__ float nudge(float a) { return __uint_as_float(__float_as_uint(a) + 0x8000u); }
__device__ __forceinline__ u32x4 even8(f32x4 a, f32x4 b) {
  return u32x4{pk(nudge(a[0]), a[1]), pk(nudge(a[2]), a[3]), pk(nudge(b[0]), b[1]), pk(nudge(b[2]), b[3])} & 0xfffffffeu;
}
__device__ __forceinline__ unsigned odd2(float a, float b) { return __builtin_amdgcn_perm(pk(a, b), __float_as_uint(a), 0x07060302u) | 1u; }
__device__ __forceinline__ u32x4 odd8(f32x4 a, f32x4 b) { return u32x4{odd2(a[0], a[1]), odd2(a[2], a[3]), odd2(b[0], b[1]), odd2(b[2], b[3])}; }
__device__ __forceinline__ u32x4 blank() {
  u32x4 o;
  asm volatile("v_mov_b32 %0, -1" : "=v"(o.x));
  asm volatile("v_mov_b32 %0, -1" : "=v"(o.y));
  asm volatile("v_mov_b32 %0, -1" : "=v"(o.z));
  asm volatile("v_mov_b32 %0, -1" : "=v"(o.w));
  return o;
}
__device__ __forceinline__ unsigned top(unsigned m, u32x4 v) { return max(max(m, max(v.x, v.y)), max(v.z, v.w)); }
__device__ __forceinline__ bool absent(unsigned m) { return __builtin_amdgcn_ballot_w64(m == 0xffffffffu) != 0; }
__device__ __forceinline__ void mac(f32x4& c, u32x4 a, u32x4 b, bool first) {
  if (first) asm("v_mfma_f32_16x16x32_bf16 %0, %1, %2, 0" : "=&v"(c) : "v"(a), "v"(b));
  else asm("v_mfma_f32_16x16x32_bf16 %0, %1, %2, %0" : "+v"(c) : "v"(a), "v"(b));
}
__device__ __forceinline__ void pickers(u32x4& lo, u32x4& hi) {
  const int l = fresh_lane();
  const unsigned v = (l >> 4) == ((l >> 2) & 3) ? 0x3f80u << ((l & 1) * 16) : 0u, d0 = (l & 2) ? 0u : v, d1 = (l & 2) ? v : 0u;
  lo = u32x4{d0, d1, 0u, 0u};
  hi = u32x4{0u, 0u, d0, d1};
  PIN(lo);
  PIN(hi);
  SCHED();
  asm volatile("s_nop 1");
  SCHED();
}
#define MFMA_DONE() do { SCHED(); asm volatile("s_nop 7"); SCHED(); } while (0)
template <int C>
__device__ __forceinline__ float dpp(float v) { return __uint_as_float(__builtin_amdgcn_update_dpp(0, __float_as_uint(v), C, 0xf, 0xf, false)); }
template <int C>
__device__ __forceinline__ float dppadd(float v) { return v + dpp<C>(v); }
__device__ __forceinline__ float swap16add(float a, float b) {
  auto r = __builtin_amdgcn_permlane16_swap(__float_as_uint(a), __float_as_uint(b), false, false);
  return __uint_as_float(r[0]) + __uint_as_float(r[1]);
}
__device__ __forceinline__ float swap32add(float a, float b) {
  auto r = __builtin_amdgcn_permlane32_swap(__float_as_uint(a), __float_as_uint(b), false, false);
  return __uint_as_float(r[0]) + __uint_as_float(r[1]);
}
__device__ __forceinline__ float sum16(float v) {
  v = dppadd<0x128>(dppadd<0x124>(v));
  v = swap16add(v, v);
  return swap32add(v, v);
}
__device__ __forceinline__ float colsums(f32x4 a, f32x4 b, bool hi8) {
  const float t0 = swap32add(a[0], b[0]), t1 = swap32add(a[1], b[1]), t2 = swap32add(a[2], b[2]), t3 = swap32add(a[3], b[3]);
  const float u0 = swap16add(t0, t2), u1 = swap16add(t1, t3);
  return (hi8 ? u1 : u0) + dpp<0x128>(hi8 ? u0 : u1);
}
__device__ __forceinline__ float rowsums(const float (&s)[9], bool hi8) {
  const float z = colsums(f32x4{s[0], s[1], s[2], s[3]}, f32x4{s[4], s[5], s[6], s[7]}, hi8) + s[8];
  return dppadd<0x141>(dppadd<0x4e>(dppadd<0xb1>(z)));
}
__device__ __forceinline__ void fst(char* p, u64 v) { __hip_atomic_store((gu64*)p, v, __ATOMIC_RELAXED, __HIP_MEMORY_SCOPE_AGENT); }
__device__ __forceinline__ void lds_wait(LAS char* p, unsigned v) {
  _Pragma("unroll 1") for (int it = 0; it < SPIN_CAP * 16; ++it) {
    if (__hip_atomic_load((LAS unsigned*)p, __ATOMIC_RELAXED, __HIP_MEMORY_SCOPE_WORKGROUP) == v) break;
    __builtin_amdgcn_s_sleep(1);
  }
  asm volatile("" ::: "memory");
}
__device__ __forceinline__ unsigned lds_add(LAS char* p) {
  asm volatile("s_waitcnt lgkmcnt(0)" ::: "memory");
  return __hip_atomic_fetch_add((LAS unsigned*)p, 1u, __ATOMIC_RELAXED, __HIP_MEMORY_SCOPE_WORKGROUP);
}
__device__ __forceinline__ void owner(int f, int& w, int& j) {
  w = (f * 43) >> 7;
  const int r = f - 3 * w;
  j = (w & 1) ? (r == 0 ? 2 : r - 1) : r;
}
constexpr int due(int j) { return (j * DUE) / 89; }
constexpr int issued(int it) {
  int n = 0;
  for (int j = 0; j < 90; ++j) n += due(j) < it;
  return n;
}
constexpr int X_W2 = 5, X_OP = 80;
constexpr int extra(int it) { return (it == X_W2) + (it == X_OP); }
constexpr int younger(int it) {
  int n = issued(it) - (it - 18) - 1;
  for (int s = due(it - 18); s < it; ++s) n += extra(s);
  return n;
}

#define STAMP(i)
#define STAMPX(i)
#define STAMPM(i)

__global__ __launch_bounds__(768, 3) void fused_block(const u16* __restrict__ x, const u16* __restrict__ wre, const u16* __restrict__ wim,
                                                      const u16* __restrict__ dre, const u16* __restrict__ dim, const u16* __restrict__ w1,
                                                      const u16* __restrict__ w2, const u16* __restrict__ cvec, const u16* __restrict__ wmod,
                                                      char* __restrict__ ws, u16* __restrict__ out) {
  extern __shared__ __attribute__((aligned(16))) char smem[];
  LAS char* L = (LAS char*)smem;
  const unsigned lb = (unsigned)(size_t)L;
  const int w = __builtin_amdgcn_readfirstlane(threadIdx.x >> 6);
  const int b = blockIdx.x, x8 = b & 7, idx = b >> 3, g = idx & 3, j8 = idx >> 2;
  const int mat = x8 >> 2, rta = 8 * (x8 & 3) + (FLIP ? (j8 + (((2 * x8 - g) & 3) == 3 ? 2 : 6)) & 7 : j8);
  const int kq4 = x8 >> 1, kh = kq4 & 1, R = 8 * (x8 & 1) + j8;
  const int rt = 2 * R + (g >> 1), sl = 2 * kq4 + (g & 1), pc = rt * 8 + sl;
  const int si = rt & 15;
  const int kq = w & 3, cg = w >> 2, crot = CROT == 1 ? g : (CROT == 2 ? j8 : (CROT == 3 ? j8 + (g >> 1) * 2 : (CROT == 4 ? g + j8 : 0)));
  const bool job = idx < 27;
  const rsrc_t rWs = mkrs(ws, WS_END);
  const u32x4 sWs = mksrd(ws, WS_END), sS = mksrd(mat ? wim : wre, S * S * 2);
  const int abown = AB_OFF + ((mat * 4 + g) * 32 + rta) * 18432;
  if (threadIdx.x < 3) *(LAS u32x4*)(L + C_MP + threadIdx.x * 16) = u32x4{0u, 0u, 0u, 0u};
  BAR();
  STAMP(0);
  auto ws_dma = [&](auto NT, int c, int n, int vdma) {
    dma<NT>(lb + (c & 1) * 65536 + n * 1024, vdma, sS, (128 * R + 8 * ((n & 15) ^ ((n >> 4) << 2))) * 4096 + kh * 2048 + ((c + crot) & 3) * 512 + (n >> 4) * 128);
  };
  auto mk_vdma = [&]() {
    const int lane = fresh_lane();
    return (lane >> 3) * 4096 + (((lane & 7) ^ ((4 * (w & 1) + (lane >> 4)) & 7)) * 16);
  };
  u64 tag;
  auto get_tag = [&]() {
    const u32x2 t = *(LAS u32x2*)(L + L_CT);
    tag = ((u64)(unsigned)__builtin_amdgcn_readfirstlane(t.y) << 32) | (unsigned)__builtin_amdgcn_readfirstlane(t.x);
  };
  auto await = [&](u32x2& v, int voff) {
    int n = SPIN_CAP;
    asm volatile(
        "1:\n\tv_cmp_ne_u64 vcc, %[tg], %[v]\n\ts_cbranch_vccz 2f\n\ts_sleep 2\n\tbuffer_load_dwordx2 %[v], %[vo], %[rs], 0 offen sc1\n\t"
        "s_sub_u32 %[n], %[n], 1\n\ts_waitcnt vmcnt(0)\n\ts_cbranch_scc0 1b\n2:"
        : [v] "+v"(v), [n] "+s"(n)
        : [tg] "s"(tag), [vo] "v"(voff), [rs] "s"(sWs)
        : "vcc", "scc", "memory");
  };
  u32x4 V[9], Wd[18], cw;
  u32x2 ev;
  unsigned cx;
  const int jm = x8 * 5 + idx - 27;
  const bool mjob = job ? b < 36 : jm < 36;
  const u32x4 sC = mksrd(cvec, D * 2), sD = mksrd(mat ? dim : dre, D * D * 2);
  const int wr = (w + 2 * g) & 7;
  unsigned wm;
  __builtin_amdgcn_s_setprio(2);
  if (w >= 4) {
    const int lane = fresh_lane();
    const u32x4 sX = mksrd(x, S * D * 2);
    const int so = (64 * rta + 8 * wr) * 1152;
#pragma unroll
    for (int r = 0; r < 8; ++r) fill<XP>(V[r], lane * 16, sX, so + r * 1152);
    fill<XP>(V[8], (lane >> 3) * 1152 + 1024 + (lane & 7) * 16, sX, so);
  } else {
    const int lane = fresh_lane(), rb = (job ? 288 * (b & 1) : 0) + 72 * w;
    fill64<1>(ev, 0, sWs, E_OFF);
    asm volatile("buffer_load_dword %0, %1, %2, %3 offen sc1" : "=v"(wm) : "v"(TCH && idx >= 5 && w ? lane * 128 : OOB), "s"(sD), "s"(((idx - 5) * 3 + w - 1) * 8192) : "memory");
    if (mjob) {
      const u32x4 sM = mksrd(wmod, D * 6 * D * 2);
      const int so = rb * 6912 + (job ? (b >> 1) * 128 : 2304 + jm * 128);
      fill<0>(cw, (lane >> 3) * 16, sC, rb * 2);
      asm volatile("buffer_load_ushort %0, %1, %2, %3 offen" : "=v"(cx) : "v"((lane >> 3) * 2), "s"(sC), "s"(rb * 2 + 128) : "memory");
#pragma unroll
      for (int i = 0; i < 8; ++i) fill<2>(V[i], (lane >> 3) * 8 * 6912 + (lane & 7) * 16, sM, so + i * 6912);
      fill<2>(V[8], (lane >> 3) * 6912 + (lane & 7) * 16, sM, so + 64 * 6912);
    }
  }
  __builtin_amdgcn_s_setprio(0);
  unsigned ipf;
  {
    u64 pc0;
    unsigned len;
    asm volatile("s_getpc_b64 %0\n.Lfb_lo:\n\ts_mov_b32 %1, .Lfb_hi-.Lfb_lo" : "=s"(pc0), "=s"(len));
    const u32x4 sI = {(unsigned)pc0, (unsigned)(pc0 >> 32) & 0xffffu, len, 0x00020000u};
    asm volatile("buffer_load_dword %0, %1, %2, 0 offen" : "=v"(ipf) : "v"(w == 3 ? fresh_lane() * 128 + (idx & 7) * 8192 : OOB), "s"(sI) : "memory");
  }
  STAMP(6);
  auto wd_dma = [&](int q0, auto NQ) {
    constexpr int nq = NQ;
    const int lane = fresh_lane(), f = 9 * g + (w < 8 ? w : 8);
    const int vo = (8 * (lane >> 5) + ((lane >> 3) & 3)) * 1152 + (((lane & 7) ^ (lane >> 4)) * 16);
    const int so = (32 * (f >> 1) + 4 * (f & 1)) * 1152 + q0 * 128;
#pragma unroll
    for (int i = 0; i < nq; ++i) {
      dma<(WDP == 2)>(lb + w * 10240 + i * 2048, vo, sD, so + i * 128);
      dma<(WDP == 2)>(lb + w * 10240 + i * 2048 + 1024, vo ^ 64, sD, so + i * 128 + 16 * 1152);
    }
  };
  auto wd_get = [&](auto K0, auto N) {
    constexpr int k0 = K0, n = N;
    const int lane = fresh_lane();
    const int p = w * 10240 + (lane & 15) * 128 + (((lane >> 4) ^ ((lane >> 1) & 7)) * 16);
    const int pe = w >= 10 ? p ^ 64 : p, po = w >= 10 ? p + 2048 : p ^ 64;
#pragma unroll
    for (int i = 0; i < n; ++i) Wd[k0 + i] = *(LAS u32x4*)(L + ((i & 1) ? po : pe) + (i >> 1) * 2048);
#pragma unroll
    for (int i = 0; i < n; ++i) PIN(Wd[k0 + i]);
  };
  wd_dma(w >= 10 ? 4 : 0, ic<5>{});
  STAMP(7);
  auto silu = [&](float c) { return pk(c * __builtin_amdgcn_rcpf(1.f + __expf(-c)), 0.f); };
  unsigned sv[9];
  asm volatile("s_waitcnt vmcnt(%2)" : "+v"(cw), "+v"(cx) : "i"(19 + IPF));
  if (w < 4) {
#pragma unroll
    for (int i = 0; i < 9; ++i) {
      const unsigned u = i < 8 ? cw[i >> 1] : cx;
      sv[i] = silu((i & 1) ? bfhi(u) : bflo(u));
      PIN(sv[i]);
    }
  }
  asm volatile("s_waitcnt vmcnt(10)"
               : "+v"(V[0]), "+v"(V[1]), "+v"(V[2]), "+v"(V[3]), "+v"(V[4]), "+v"(V[5]), "+v"(V[6]), "+v"(V[7]), "+v"(V[8]), "+v"(ev), "+v"(wm));
  PIN(ipf);
  STAMP(1);
  if (w >= 4) {
    const int lane = fresh_lane();
    float s1[9];
    unsigned lo = 0x3f80u, hi = 0x3f800000u;
    PIN(lo);
    PIN(hi);
#pragma unroll
    for (int r = 0; r < 9; ++r) s1[r] = dot2(V[r].w, 0x3f803f80u, dot2(V[r].z, 0x3f803f80u, dot2(V[r].y, 0x3f803f80u, dot2(V[r].x, 0x3f803f80u, 0.f))));
    float nm = rowsums(s1, lane & 8) * (-1.f / D);
    PIN(nm);
#pragma unroll
    for (int r = 0; r < 9; ++r) {
      float e[8];
#define CENTRE(CON, m)                                                                                                 \
  asm volatile(                                                                                                        \
      "s_nop 1\n\tv_dot2_f32_bf16 %0, %8, %12, %14\n\tv_dot2_f32_bf16 %1, %8, %13, %14\n\tv_dot2_f32_bf16 %2, %9, %12, %14\n\t" \
      "v_dot2_f32_bf16 %3, %9, %13, %14\n\tv_dot2_f32_bf16 %4, %10, %12, %14\n\tv_dot2_f32_bf16 %5, %10, %13, %14\n\t" \
      "v_dot2_f32_bf16 %6, %11, %12, %14\n\tv_dot2_f32_bf16 %7, %11, %13, %14\n\ts_nop 2"                              \
      : "=&v"(e[0]), "=&v"(e[1]), "=&v"(e[2]), "=&v"(e[3]), "=&v"(e[4]), "=&v"(e[5]), "=&v"(e[6]), "=&v"(e[7])         \
      : "v"(V[r].x), "v"(V[r].y), "v"(V[r].z), "v"(V[r].w), "v"(lo), "v"(hi), CON(m))
      if (r < 8) CENTRE("s", (__builtin_amdgcn_readlane(__float_as_uint(nm), 8 * (r & 7))));
      else CENTRE("v", (nm));
      const f32x2 d0 = {e[0], e[1]}, d1 = {e[2], e[3]}, d2 = {e[4], e[5]}, d3 = {e[6], e[7]};
      const f32x2 t = d0 * d0 + (d1 * d1 + (d2 * d2 + d3 * d3));
      s1[r] = t.x + t.y;
    }
    float rs = rsqrtf(rowsums(s1, lane & 8) * (1.f / D) + 1e-5f), sh = nm * rs;
    PIN(rs);
    PIN(sh);
    STAMP(2);
    VMW(0);
    wd_get(ic<0>{}, ic<10>{});
    if (w < 8) {
      wd_dma(5, ic<4>{});
    } else {
      const u32x4 ones = blank();
      const int vl = (CLR & 4) ? fresh_lane() * 16 : OOB;
#pragma unroll
      for (int i = 0; i < 5; ++i) cst128(ones, rWs, w - 8 + 4 * i < 18 ? vl : OOB, abown + (w - 8 + 4 * i) * 1024);
    }
    STAMP(29);
#pragma unroll
    for (int r = 0; r < 9; ++r) {
      const float a = r < 8 ? __uint_as_float(__builtin_amdgcn_readlane(__float_as_uint(rs), 8 * (r & 7))) : rs;
      const float c = r < 8 ? __uint_as_float(__builtin_amdgcn_readlane(__float_as_uint(sh), 8 * (r & 7))) : sh;
      V[r] = pk8(up4(V[r].x, V[r].y) * a + c, up4(V[r].z, V[r].w) * a + c);
      PIN(V[r]);
    }
    if (w < 8) VMW(0);
  } else {
    const int lane = fresh_lane();
    tag = (((u64)(unsigned)__builtin_amdgcn_readfirstlane(ev.y) << 32) | (unsigned)__builtin_amdgcn_readfirstlane(ev.x)) + 1;
    const unsigned tl = (unsigned)tag;
    if (w == 0 && lane == 0) *(LAS u64*)(L + L_CT) = tag;
    bool last = false;
    f32x2 v;
    if (mjob) {
      f32x4 a0 = {0.f, 0.f, 0.f, 0.f}, a1 = a0;
#pragma unroll
      for (int i = 0; i < 9; ++i) axpy8(a0, a1, V[i], sv[i], sv[i] << 16);
      *(LAS float*)(L + L_MP + w * 256 + (lane & 7) * 32 + (lane >> 3) * 4) = colsums(a0, a1, lane & 8);
      unsigned n = 0;
      if (lane == 0) n = lds_add(L + C_MP);
      last = __builtin_amdgcn_readfirstlane(n) == 3;
      asm volatile("" ::: "memory");
      LAS char* p = L + L_MP + (lane & 31) * 8;
      v = (*(LAS f32x2*)p + *(LAS f32x2*)(p + 256)) + (*(LAS f32x2*)(p + 512) + *(LAS f32x2*)(p + 768));
      if (last && !job) *(LAS f32x2*)p = v;
      last &= job;
    }
#pragma unroll
    for (int i = 0; i < 4; ++i)
      cst128(u32x4{__float_as_uint(v.x), tl, __float_as_uint(v.y), tl}, rWs, last ? (lane >> 5) * 18432 + (lane & 31) * 16 : OOB,
             MG_OFF + i * 36864 + (b & 1) * 9216 + (b >> 1) * 512);
    STAMP(2);
    VMW(4);
    wd_get(ic<0>{}, ic<10>{});
    wd_dma(5, ic<4>{});
    STAMP(29);
    const int mo = MG_OFF + x8 * 18432 + w * 1024, vl = lane * 16, v2 = w == 0 ? vl : OOB;
    u32x4 p[6];
    _Pragma("unroll 1") for (int it = 0; it < SPIN_CAP; ++it) {
#pragma unroll
      for (int i = 0; i < 6; ++i) fill<1>(p[i], i < 4 ? vl : v2, sWs, mo + (i >> 1) * 4096 + (i & 1) * 9216);
      asm volatile("s_waitcnt vmcnt(0)" : "+v"(p[0]), "+v"(p[1]), "+v"(p[2]), "+v"(p[3]), "+v"(p[4]), "+v"(p[5]));
      unsigned d = 0, e = 0;
#pragma unroll
      for (int i = 0; i < 4; ++i) d |= (p[i].y ^ tl) | (p[i].w ^ tl);
#pragma unroll
      for (int i = 4; i < 6; ++i) e |= (p[i].y ^ tl) | (p[i].w ^ tl);
      if (__builtin_amdgcn_ballot_w64((w == 0 ? d | e : d) != 0) == 0) break;
      __builtin_amdgcn_s_sleep(2);
    }
#pragma unroll
    for (int i = 0; i < 3; ++i) {
      unsigned o = pk(__uint_as_float(p[2 * i].x) + __uint_as_float(p[2 * i + 1].x), __uint_as_float(p[2 * i].z) + __uint_as_float(p[2 * i + 1].z));
      if ((w + 4 * i) * 64 + lane >= 288) o = pk(bflo(o) + 1.f, bfhi(o) + 1.f);
      if (i < 2 || w == 0) *(LAS f32x2*)(L + L_SA + (w + 4 * i) * 512 + lane * 8) = f32x2{bflo(o), bfhi(o)};
    }
  }
  STAMP(30);
  u32x4 Wr[4];
  u32x2 cv2;
  const int bn = idx * 12 + w, bph = bn >= 162, bkb = ((bn - 162 * bph) * 57) >> 9, bnb = bn - 162 * bph - 9 * bkb;
  {
    const int lane = fresh_lane();
    const int pitch = job ? (bph ? 1152 : F * 2) : 6912, step = job ? 8 * pitch : pitch;
    const int so = job ? (32 * bkb + (bph ? x8 * 576 : 0)) * pitch + (bph ? 0 : x8 * 1152) + bnb * 128 : (288 + 32 * w) * 6912 + 2304 + jm * 128;
    const u32x4 sW = mksrd(job ? (bph ? w2 : w1) : wmod, job ? D * F * 2 : (jm < 36 && w < 9 ? D * 6 * D * 2 : 0u));
    const int vo = (lane >> 3) * (job ? pitch : 4 * 6912) + (lane & 7) * 16;
#pragma unroll
    for (int i = 0; i < 4; ++i) fill<2>(Wr[i], vo, sW, so + i * step);
    fill64<0>(cv2, w < 9 ? (lane >> 3) * 8 : OOB, sC, 576 + 64 * w);
  }
  STAMP(31);
  if (w < 8) wd_get(ic<10>{}, ic<8>{});
  BAR();
  STAMP(3);
  u32x2 gv;
  if (w >= 4) {
    get_tag();
    const int lane = fresh_lane();
    LAS char* sp = L + L_SA + lane * 32;
    LAS char* sq = L + L_SA + 2048 + (lane & 7) * 32;
    const f32x4 sa0 = *(LAS f32x4*)sp, sa1 = *(LAS f32x4*)(sp + 16), ca0 = *(LAS f32x4*)(sp + 2304), ca1 = *(LAS f32x4*)(sp + 2320);
    const int hb = L_H + (wr >> 2) * 2048 + (lane & 3) * 256 + (((4 * wr) ^ (lane & 7)) & 15) * 16;
#pragma unroll
    for (int r = 0; r < 8; ++r) {
      *(LAS u32x4*)(L + ((hb + (lane >> 2) * 4096) ^ ((r & 3) * 16)) + (r >> 2) * 1024) = pk8(up4(V[r].x, V[r].y) * ca0 + sa0, up4(V[r].z, V[r].w) * ca1 + sa1);
      if (r & 1) SCHED();
    }
    const f32x4 ta0 = *(LAS f32x4*)sq, ta1 = *(LAS f32x4*)(sq + 16), da0 = *(LAS f32x4*)(sq + 2304), da1 = *(LAS f32x4*)(sq + 2320);
    *(LAS u32x4*)(L + ((hb + 65536 + (lane & 4) * 1024 + (lane >> 5) * 1024) ^ (((lane >> 3) & 3) * 16))) =
        pk8(up4(V[8].x, V[8].y) * da0 + ta0, up4(V[8].z, V[8].w) * da1 + ta1);
    VMW(5);
  } else {
    const int vdma = mk_vdma();
#pragma unroll
    for (int i = 0; i < 16; ++i) ws_dma(ic<C0NT>{}, 0, w + 4 * i, vdma);
  }
  BAR();
  STAMP(4);
  {
    const int lane = fresh_lane();
    const int ra = L_H + (lane >> 4) * 256 + (((lane & 15) ^ (lane >> 4)) * 16);
    const unsigned neg = mat ? 0x80008000u : 0u;
    int fw, fj;
    owner(9 * g + (w < 8 ? w : 8), fw, fj);
    const int so = abown + (fw - 3 * g) * 6144 + fj * 1024;
    f32x4 c[4];
    if (w < 8) {
      if (w == 4 && lane == 0) fst(ws + G0_OFF + ((mat * 4 + g) * 32 + rta) * 8, tag);
      u32x4 Hf[2][4];
#pragma unroll
      for (int rb = 0; rb < 4; ++rb) Hf[0][rb] = *(LAS u32x4*)(L + ra + rb * 1024);
      sfor<18>([&](auto KB) {
        constexpr int kb = KB;
        if (kb < 17) {
#pragma unroll
          for (int rb = 0; rb < 4; ++rb) Hf[(kb + 1) & 1][rb] = *(LAS u32x4*)(L + (ra ^ ((kb + 1) & 1) * 64) + (kb + 1) * 4096 + rb * 1024);
        }
        SCHED();
#pragma unroll
        for (int rb = 0; rb < 4; ++rb) mac(c[rb], Hf[kb & 1][rb], Wd[kb], kb == 0);
        SCHED();
      });
      MFMA_DONE();
      cst128(even8(c[0], c[1]) ^ neg, rWs, lane * 16, so);
      cst128(even8(c[2], c[3]) ^ neg, rWs, lane * 16, so + 3072);
      VMW_SEL(2, 16, 4, );
    } else {
      const int g0 = G0_OFF + ((mat * 4 + g) * 32 + 16 * kh + (lane & 15)) * 8;
      fill64<1>(gv, w == 11 ? g0 : OOB, sWs, 0);
      const int rk = (ra ^ (w >= 10 ? 64 : 0)) + (w >= 10 ? 9 * 4096 : 0) + (w & 1) * 2048;
      u32x4 Hf[4][2];
      auto ldh = [&](int i) {
        Hf[i & 3][0] = *(LAS u32x4*)(L + (rk ^ (i & 1) * 64) + i * 4096);
        Hf[i & 3][1] = *(LAS u32x4*)(L + (rk ^ (i & 1) * 64) + i * 4096 + 1024);
      };
      ldh(0);
      ldh(1);
      ldh(2);
#pragma unroll
      for (int i = 0; i < 9; ++i) {
        if (i < 6) ldh(i + 3);
        SCHED();
        mac(c[0], Hf[i & 3][0], Wd[i], i == 0);
        mac(c[1], Hf[i & 3][1], Wd[i], i == 0);
        SCHED();
      }
      MFMA_DONE();
      LAS char* pp = L + L_PR + (w & 1) * 2048 + lane * 16;
      if (w >= 10) {
        *(LAS f32x4*)pp = c[0];
        *(LAS f32x4*)(pp + 1024) = c[1];
        if (lane == 0) lds_add(L + C_PR + (w & 1) * 4);
        asm volatile("s_waitcnt vmcnt(0)" : "+v"(gv));
        if (w == 11) await(gv, g0);
      } else {
        lds_wait(L + C_PR + (w & 1) * 4, 1);
        cst128(even8(c[0] + *(LAS f32x4*)pp, c[1] + *(LAS f32x4*)(pp + 1024)) ^ neg, rWs, lane * 16, so + (w & 1) * 3072);
        asm volatile("s_waitcnt vmcnt(1)" : "+v"(gv));
      }
    }
  }
#pragma unroll
  for (int i = 0; i < 4; ++i) PIN(Wr[i]);
  PIN(cv2);
  STAMP(5);
  BAR();
  int vl16, rd0;
  {
    const int lane = fresh_lane();
    vl16 = lane * 16;
    rd0 = kq * 16384 + (lane & 15) * 128 + (((lane >> 4) ^ ((lane & 15) >> 1)) * 16);
    LAS char* sp = L + L_H + w * 4096;
    if (job) {
#pragma unroll
      for (int i = 0; i < 4; ++i) *(LAS u32x4*)(sp + i * 1024 + vl16) = Wr[i];
      LAS char* rp = sp + ((lane >> 4) * 8 + ((lane >> 2) & 3)) * 128 + (lane & 3) * 16;
#pragma unroll
      for (int i = 0; i < 4; ++i) {
        const u32x2 lo = __builtin_bit_cast(u32x2, __builtin_amdgcn_ds_read_tr16_b64_v4i16((LAS s16x4*)(rp + (i >> 1) * 64 + (i & 1) * 8)));
        const u32x2 hi = __builtin_bit_cast(u32x2, __builtin_amdgcn_ds_read_tr16_b64_v4i16((LAS s16x4*)(rp + (i >> 1) * 64 + (i & 1) * 8 + 512)));
        Wr[i] = u32x4{lo.x, lo.y, hi.x, hi.y};
      }
    } else {
      f32x4 a0 = {0.f, 0.f, 0.f, 0.f}, a1 = a0;
#pragma unroll
      for (int i = 0; i < 4; ++i) {
        const unsigned u = cv2[i >> 1], t = silu((i & 1) ? bfhi(u) : bflo(u));
        axpy8(a0, a1, Wr[i], t, t << 16);
      }
      *(LAS float*)(sp + (lane & 7) * 32 + (lane >> 3) * 4) = colsums(a0, a1, lane & 8);
      unsigned n = 0;
      if (lane == 0) n = lds_add(L + C_MV);
      const bool last = __builtin_amdgcn_readfirstlane(n) == 11;
      asm volatile("" ::: "memory");
      f32x4 v = *(LAS f32x4*)(L + L_MP + (lane & 15) * 16);
#pragma unroll
      for (int k = 0; k < 9; ++k) v += *(LAS f32x4*)(L + L_H + k * 4096 + (lane & 15) * 16);
      u32x2 o = pk4(v);
      if (jm >= 18 && jm < 27) o = pk4(up4(o.x, o.y) + 1.f);
      cst128(u32x4{o.x, (unsigned)tag, o.y, (unsigned)tag}, rWs, last && jm < 36 && lane < 16 ? vl16 : OOB, MV_OFF + jm * 256);
    }
  }
  const int abin = AB_OFF + ((mat * 4 + g) * 32 + 16 * kh + kq) * 18432 + cg * 6144;
  auto ab_at = [&](int c) { return abin + ((c + crot) & 3) * 73728; };
  u32x4 Ra[6];
  _Pragma("unroll 1") for (int it = 0; it < SPIN_CAP; ++it) {
#pragma unroll
    for (int i = 0; i < 6; ++i) fill<1>(Ra[i], vl16, sWs, ab_at(0) + i * 1024);
    asm volatile("s_waitcnt vmcnt(0)" : "+v"(Ra[0]), "+v"(Ra[1]), "+v"(Ra[2]), "+v"(Ra[3]), "+v"(Ra[4]), "+v"(Ra[5]));
    unsigned m = 0;
#pragma unroll
    for (int i = 0; i < 6; ++i) m = top(m, Ra[i]);
    if (!absent(m)) break;
  }
  STAMP(8);
#pragma unroll
  for (int i = 0; i < 4; ++i) {
    int fw, fj;
    owner(4 * bnb + i, fw, fj);
    cst128(Wr[i], rWs, job ? vl16 : OOB, WT_OFF + (x8 * 12 + fw) * 110592 + (bph * 54 + bkb * 3 + fj) * 1024);
  }
  BAR();
  STAMP(9);
  f32x4 acc[8][3];
  auto touch = [&](unsigned& r, int ph) {
    const int lane = fresh_lane();
    asm volatile("buffer_load_dword %0, %1, %2, %3 offen sc1"
                 : "=v"(r)
                 : "v"(lane < 27 && (T0 || ph) ? lane * 128 : OOB), "s"(sWs), "s"(WT_OFF + (sl * 12 + w) * 110592 + ph * 55296 + si * 3456)
                 : "memory");
  };
  {
    u32x4 Wf[2][4];
    auto ldw = [&](int s, int h) {
#pragma unroll
      for (int r = 0; r < 4; ++r) Wf[h][r] = *(LAS u32x4*)(L + ((s >> 1) & 1) * 65536 + (4 * h + r) * 2048 + (rd0 ^ ((s & 1) * 64)));
    };
    auto gw_at = [&]() { return GW_OFF + (sl * 32 + (fresh_lane() & 31)) * 8; };
    const int wq = w < 9 ? w : w - 9;
    sfor<8>([&](auto SS) {
      constexpr int s = SS, c = s >> 1, kk = s & 1;
      if constexpr (s >= 2) {
        constexpr int y = s == 7 ? 2 : (kk ? (s == 3 ? 11 : 9) : 3);
        asm volatile("s_waitcnt vmcnt(%3)" : "+v"(Ra[3 * kk]), "+v"(Ra[3 * kk + 1]), "+v"(Ra[3 * kk + 2]) : "i"(y));
      }
      int vdma;
      if constexpr (kk == 0) {
        if constexpr (s > 0) BAR();
        STAMP(10 + c);
        if constexpr (s == 4) {
          u32x4 mv = *(LAS u32x4*)(L + L_X2 + wq * 1024 + vl16);
          ldw(s, 0);
          _Pragma("unroll 1") for (int it = 0; it < SPIN_CAP; ++it) {
            if (__builtin_amdgcn_ballot_w64(((mv.y ^ (unsigned)tag) | (mv.w ^ (unsigned)tag)) != 0) == 0) break;
            refill(mv, vl16, sWs, MV_OFF + wq * 1024);
            asm volatile("s_waitcnt vmcnt(0)" : "+v"(mv));
          }
          *(LAS f32x4*)(L + L_MV + wq * 1024 + vl16) = up4(mv.x, mv.z);
          SCHED();
        } else {
          ldw(s, 0);
        }
        ldw(s, 1);
        SCHED();
        if constexpr (c < 3) vdma = mk_vdma();
        if constexpr (s == 2) {
          if (w == 11 && fresh_lane() == 0) fst(ws + GW_OFF + (x8 * 32 + idx) * 8, tag);
          dma<2>(lb + L_X2 + wq * 1024, vl16, sWs, MV_OFF + wq * 1024);
        }
        if constexpr (s == 4) {
          if (w == 11 && fresh_lane() == 0) fst(ws + G1_OFF + pc * 8, tag);
          fill64<1>(gv, gw_at(), sWs, 0);
        }
        if constexpr (s == 6) {
          PIN(gv);
          await(gv, gw_at());
          touch(wm, 0);
          fill64<1>(gv, G1_OFF + (16 * R + (fresh_lane() & 15)) * 8, sWs, 0);
        }
      }
      if constexpr (s >= 2) {
        if (absent(top(top(top(0u, Ra[3 * kk]), Ra[3 * kk + 1]), Ra[3 * kk + 2]))) {
          _Pragma("unroll 1") for (int it = 0; it < SPIN_CAP; ++it) {
#pragma unroll
            for (int j = 0; j < 3; ++j) refill(Ra[3 * kk + j], vl16, sWs, ab_at(c) + kk * 3072 + j * 1024);
            asm volatile("s_waitcnt vmcnt(0)" : "+v"(Ra[3 * kk]), "+v"(Ra[3 * kk + 1]), "+v"(Ra[3 * kk + 2]));
            if (!absent(top(top(top(0u, Ra[3 * kk]), Ra[3 * kk + 1]), Ra[3 * kk + 2]))) break;
          }
        }
      }
      SCHED();
      sfor<3>([&](auto J) {
        constexpr int j = J;
#pragma unroll
        for (int r = 0; r < 4; ++r) mac(acc[r][j], Ra[3 * kk + j], Wf[0][r], s == 0);
        SCHED();
        if constexpr (kk == 0 && c < 3) ws_dma(ic<0>{}, c + 1, w + 12 * (2 * j), vdma);
        if constexpr (kk == 0 && s < 4 && j < 2)
          cst128(blank(), rWs, (CLR & 1) && (s < 2 || j < 1 || w < 9) ? vl16 : OOB, XB_OFF + pc * 46080 + (w + 12 * (s + j)) * 1024);
        if constexpr (kk == 0 && j == 2) ldw(s + 1, 0);
        SCHED();
#pragma unroll
        for (int r = 0; r < 4; ++r) mac(acc[4 + r][j], Ra[3 * kk + j], Wf[1][r], s == 0);
        SCHED();
        if constexpr (kk == 0 && j == 2) ldw(s + 1, 1);
        if constexpr (c < 3) refill(Ra[3 * kk + j], vl16, sWs, ab_at(c + 1) + kk * 3072 + j * 1024);
        if constexpr (kk == 0 && c < 3) {
          if constexpr (j < 2) ws_dma(ic<0>{}, c + 1, w + 12 * (2 * j + 1), vdma);
          else if (w < 4) ws_dma(ic<0>{}, c + 1, 60 + w, vdma);
        }
        SCHED();
      });
    });
  }
  MFMA_DONE();
  __builtin_amdgcn_s_setprio(1);
  STAMP(14);
  auto fin_map = [&](int lane, int& flo, int& fhi, bool& alo, bool& ahi) {
    const int s = lane >> 4, f2 = 3 * w + ((w & 1) ? 0 : 2);
    flo = 8 * ((lane >> 2) & 3) + (s < 2 ? 32 * ((3 * w + 1) >> 1) : 32 * (f2 >> 1) + 4 * (f2 & 1));
    fhi = flo + (s < 2 ? 4 : 0);
    alo = s == 1;
    ahi = s >= 1;
  };
  const int row0 = 32 * (pc >> 2) + 4 * (pc & 3);
  const int rot = ROT ? (si * 18) >> 4 : 0, wb = WT_OFF + (sl * 12 + w) * 110592;
  auto w_at = [&](int it) {
    const int ph = it / 54, k = (it - 54 * ph) / 3 + rot;
    return wb + ph * 55296 + (k >= 18 ? k - 18 : k) * 3072 + (it % 3) * 1024;
  };
  u32x4 Rw[18];
  {
    auto area = [&](int v) { return v < 7 ? v * 9216 : (v == 7 ? 131072 : 65536 + (v - 8) * 16384); };
    u32x4 t[9], plo, phi;
#pragma unroll
    for (int i = 1; i < 4; ++i) {
      t[3 * i - 3] = pk8(acc[2 * i][0], acc[2 * i][1]);
      t[3 * i - 2] = pk8(acc[2 * i + 1][0], acc[2 * i + 1][1]);
      t[3 * i - 1] = pk8(acc[2 * i][2], acc[2 * i + 1][2]);
    }
    STAMPX(1);
    if (w >= 8) lds_wait(L + C_Q + kq * 4, 2);
    else if (vl16 == 0) lds_add(L + C_Q + kq * 4);
    STAMPX(2);
#pragma unroll
    for (int i = 0; i < 9; ++i) *(LAS u32x4*)(L + area(w) + i * 1024 + vl16) = t[i];
    STAMPX(3);
    if (vl16 == 0) lds_add(L + C_R + cg * 4);
    STAMPX(4);
    const int lane = fresh_lane(), v48 = lane < 48 ? vl16 : OOB;
    int flo, fhi;
    bool alo, ahi;
    fin_map(lane, flo, fhi, alo, ahi);
    u32x2 xa, xb;
    {
      const u32x4 sX = mksrd(x, S * D * 2);
      fill64<0>(xa, lane < 48 ? (16 * alo + (lane & 3)) * 1152 + flo * 2 : OOB, sX, row0 * 1152);
      fill64<0>(xb, lane < 48 ? (16 * ahi + (lane & 3)) * 1152 + fhi * 2 : OOB, sX, row0 * 1152);
    }
    STAMPX(5);
    pickers(plo, phi);
    STAMPX(6);
    lds_wait(L + C_R + cg * 4, 4);
    STAMP(15);
#pragma unroll
    for (int i = 0; i < 9; ++i) t[i] = *(LAS u32x4*)(L + area(w ^ (i / 3 + 1)) + i * 1024 + vl16);
#pragma unroll
    for (int i = 0; i < 3; ++i) {
      mac(acc[0][0], plo, t[3 * i], false);
      mac(acc[0][1], phi, t[3 * i], false);
      mac(acc[1][0], plo, t[3 * i + 1], false);
      mac(acc[1][1], phi, t[3 * i + 1], false);
      mac(acc[0][2], plo, t[3 * i + 2], false);
      mac(acc[1][2], phi, t[3 * i + 2], false);
    }
    MFMA_DONE();
    STAMPX(7);
    asm volatile("s_waitcnt vmcnt(2)" : "+v"(gv), "+v"(wm));
    await(gv, G1_OFF + (16 * R + (lane & 15)) * 8);
    STAMPX(8);
    const int vpub = ((lane >> 2) & 3) * 46080 + (lane >> 4) * 64 + (lane & 3) * 16;
    const int yb = XB_OFF + (4 * R + kq) * 4 * 46080 + kq4 * 9216 + (3 * g + cg) * 768;
    cst128(even8(acc[0][0], acc[0][1]), rWs, vpub, yb);
    cst128(even8(acc[1][0], acc[1][1]), rWs, vpub, yb + 256);
    cst128(even8(acc[0][2], acc[1][2]), rWs, vpub, yb + 512);
    STAMP(16);
    u32x4 yp[4];
    __builtin_amdgcn_s_setprio(0);
#pragma unroll
    for (int i = 0; i < 9; ++i) fill<1>(Rw[i], vl16, sWs, w_at(i));
#pragma unroll
    for (int p = 0; p < 4; ++p) fill<1>(yp[p], v48, sWs, XB_OFF + pc * 46080 + p * 9216 + w * 768);
    __builtin_amdgcn_s_setprio(1);
    STAMP(17);
    asm volatile("s_waitcnt vmcnt(0)" : "+v"(yp[0]), "+v"(yp[1]), "+v"(yp[2]), "+v"(yp[3]), "+v"(xa), "+v"(xb), "+v"(wm));
    _Pragma("unroll 1") for (int it = 0; it < SPIN_CAP; ++it) {
      if (!absent(top(top(top(top(0u, yp[0]), yp[1]), yp[2]), yp[3]))) break;
      __builtin_amdgcn_s_sleep(1);
#pragma unroll
      for (int p = 0; p < 4; ++p) refill(yp[p], v48, sWs, XB_OFF + pc * 46080 + p * 9216 + w * 768);
      asm volatile("s_waitcnt vmcnt(0)" : "+v"(yp[0]), "+v"(yp[1]), "+v"(yp[2]), "+v"(yp[3]));
    }
    STAMP(18);
    f32x4 yl, yh;
    SCHED();
#pragma unroll
    for (int p = 0; p < 4; ++p) {
      mac(yl, plo, yp[p], p == 0);
      mac(yh, phi, yp[p], p == 0);
    }
    SCHED();
    const f32x4 gal = *(LAS f32x4*)(L + L_MV + flo * 4), gah = *(LAS f32x4*)(L + L_MV + fhi * 4);
    const f32x4 sbl = *(LAS f32x4*)(L + L_MV + 2304 + flo * 4), sbh = *(LAS f32x4*)(L + L_MV + 2304 + fhi * 4);
    const f32x4 cbl = *(LAS f32x4*)(L + L_MV + 4608 + flo * 4), cbh = *(LAS f32x4*)(L + L_MV + 4608 + fhi * 4);
    MFMA_DONE();
    const u32x4 x2 = pk8(up4(xa.x, xa.y) + gal * yl, up4(xb.x, xb.y) + gah * yh);
    yl = up4(x2.x, x2.y);
    yh = up4(x2.z, x2.w);
    {
      const bool on = lane < 48;
      const float lo = (yl[0] + yl[1]) + (yl[2] + yl[3]), hi = (yh[0] + yh[1]) + (yh[2] + yh[3]);
      const float ma = sum16((alo ? 0.f : lo) + (ahi ? 0.f : hi)) * (1.f / 48), mb = sum16((alo ? lo : 0.f) + (ahi ? hi : 0.f)) * (1.f / 48);
      const f32x4 dl = yl - (alo ? mb : ma), dh = yh - (ahi ? mb : ma), ql = dl * dl, qh = dh * dh;
      const float q0 = on ? (ql[0] + ql[1]) + (ql[2] + ql[3]) : 0.f, q1 = on ? (qh[0] + qh[1]) + (qh[2] + qh[3]) : 0.f;
      const float qa = sum16((alo ? 0.f : q0) + (ahi ? 0.f : q1)), qb = sum16((alo ? q0 : 0.f) + (ahi ? q1 : 0.f));
      if (lane < 4) *(LAS f32x4*)(L + L_ST + (w * 4 + lane) * 16) = f32x4{ma, mb, qa, qb};
    }
    if (lane < 48) *(LAS u32x4*)(L + L_X2 + w * 768 + vl16) = x2;
    STAMPX(9);
    BAR();
    STAMP(19);
    {
      const f32x4 e = *(LAS f32x4*)(L + L_ST + (lane < 48 ? vl16 : 0));
      const bool on = lane < 48;
      const float ma = sum16(on ? e[0] : 0.f) * (1.f / 12), mb = sum16(on ? e[1] : 0.f) * (1.f / 12);
      const float da = e[0] - ma, db = e[1] - mb;
      const float va = rsqrtf(sum16(on ? e[2] + 48.f * da * da : 0.f) * (1.f / D) + 1e-5f);
      const float vb = rsqrtf(sum16(on ? e[3] + 48.f * db * db : 0.f) * (1.f / D) + 1e-5f);
      cst128(even8((yl - (alo ? mb : ma)) * (alo ? vb : va) * cbl + sbl, (yh - (ahi ? mb : ma)) * (ahi ? vb : va) * cbh + sbh), rWs, v48,
             XB_OFF + pc * 46080 + 36864 + w * 768);
    }
    STAMPX(29);
    if (w == 11 && b == 0 && lane == 0) fst(ws + E_OFF, tag);
    u32x4 h[8];
    __builtin_amdgcn_s_setprio(0);
#pragma unroll
    for (int i = 9; i < 18; ++i) fill<1>(Rw[i], vl16, sWs, w_at(i));
#pragma unroll
    for (int p = 0; p < 8; ++p) fill<1>(h[p], v48, sWs, XB_OFF + (rt * 8 + p) * 46080 + 36864 + w * 768);
    STAMP(20);
    const int kp = (flo >> 5) - rot, lg = (lane >> 2) & 3;
    const int hl = (kp < 0 ? kp + 18 : kp) * 4096 + lg * 256 + (lg & 1) * 192 + (lane & 3) * 16 + (flo & 4) * 2;
    const int olo = hl + alo * 1024, ohi = hl + ahi * 1024 + (fhi - flo) * 2;
    asm volatile("s_waitcnt vmcnt(0)" : "+v"(h[0]), "+v"(h[1]), "+v"(h[2]), "+v"(h[3]), "+v"(h[4]), "+v"(h[5]), "+v"(h[6]), "+v"(h[7]));
    _Pragma("unroll 1") for (int it = 0; it < SPIN_CAP; ++it) {
      unsigned m = 0;
#pragma unroll
      for (int p = 0; p < 8; ++p) m = top(m, h[p]);
      if (!absent(m)) break;
      __builtin_amdgcn_s_sleep(1);
#pragma unroll
      for (int p = 0; p < 8; ++p) refill(h[p], v48, sWs, XB_OFF + (rt * 8 + p) * 46080 + 36864 + w * 768);
      asm volatile("s_waitcnt vmcnt(0)" : "+v"(h[0]), "+v"(h[1]), "+v"(h[2]), "+v"(h[3]), "+v"(h[4]), "+v"(h[5]), "+v"(h[6]), "+v"(h[7]));
    }
    STAMP(21);
    if (lane < 48) {
#pragma unroll
      for (int p = 0; p < 8; ++p) {
        *(LAS u32x2*)(L + (olo ^ ((p & 3) * 64)) + (p >> 2) * 2048) = u32x2{h[p].x, h[p].y};
        *(LAS u32x2*)(L + (ohi ^ ((p & 3) * 64)) + (p >> 2) * 2048) = u32x2{h[p].z, h[p].w};
      }
    }
  }
#pragma unroll
  for (int i = 0; i < 18; ++i) PIN(Rw[i]);
  STAMPX(30);
  BAR();
  if (w == 11 && vl16 == 0) __hip_atomic_store((LAG unsigned*)(ws + G3_OFF + pc * 4), (unsigned)tag, __ATOMIC_RELAXED, __HIP_MEMORY_SCOPE_AGENT);
  STAMP(22);
  f32x4 am[4][3];
  u32x4 g3;
  {
    sfor<2>([&](auto PH) {
      constexpr int ph = PH;
      LAS char* A = L + ph * L_H1 + (vl16 ^ ((fresh_lane() & 16) * 12));
      u32x4 Bf[2][4];
#pragma unroll
      for (int mb = 0; mb < 4; ++mb) Bf[0][mb] = *(LAS u32x4*)(A + mb * 1024);
      sfor<18>([&](auto KB) {
        constexpr int kb = KB;
        if constexpr (PL && kb == 0) __builtin_amdgcn_s_setprio(3);
        if constexpr (PL && kb == (ph ? Q1 : P1)) __builtin_amdgcn_s_setprio(2);
        if constexpr (PL && kb == (ph ? Q2 : P2)) __builtin_amdgcn_s_setprio(1);
        if constexpr (PL && kb == (ph ? Q3 : P3)) __builtin_amdgcn_s_setprio(0);
        if constexpr (kb % 3 == 0 && kb > 0) STAMPM(ph * 5 + kb / 3 == 10 ? 29 : ph * 5 + kb / 3);
        if (kb < 17) {
#pragma unroll
          for (int mb = 0; mb < 4; ++mb) Bf[(kb + 1) & 1][mb] = *(LAS u32x4*)(A + ((kb + 1) * 4 + mb) * 1024);
        }
        SCHED();
        sfor<3>([&](auto J) {
          constexpr int j = J, it = ph * 54 + kb * 3 + j;
          if constexpr (it >= 18) asm volatile("s_waitcnt vmcnt(%1)" : "+v"(Rw[it % 18]) : "i"(younger(it)));
#pragma unroll
          for (int mb = 0; mb < 4; ++mb) mac(am[mb][j], Rw[it % 18], Bf[kb & 1][mb], kb == 0);
          SCHED();
          constexpr int j0 = issued(it), j1 = issued(it + 1);
          sfor<j1 - j0>([&](auto JJ) {
            constexpr int jj = j0 + JJ;
            static_assert(jj <= it, "slot still live");
            refill(Rw[jj % 18], vl16, sWs, w_at(jj + 18));
          });
          if constexpr (it == X_W2) touch(wm, 1);
          if constexpr (it == 53) PIN(wm);
          if constexpr (it == X_OP) fill<1>(g3, vl16, sWs, G3_OFF);
          SCHED();
        });
      });
      MFMA_DONE();
      STAMP(23 + 2 * ph);
      if constexpr (ph == 0) {
        const int fp = ((3 * w + 1) >> 1) - rot, fh = ((3 * (w & ~1) + 2) >> 1) - rot;
        LAS char* Hp = A + L_H1 + (fp < 0 ? fp + 18 : fp) * 4096;
        LAS char* Hh = A + L_H1 + (fh < 0 ? fh + 18 : fh) * 4096 + (w & 1) * 8;
#pragma unroll
        for (int mb = 0; mb < 4; ++mb) {
          *(LAS u32x4*)(Hp + mb * 1024) = pk8(relu4(am[mb][0]), relu4(am[mb][1]));
          *(LAS u32x2*)(Hh + mb * 1024) = pk4(relu4(am[mb][2]));
        }
        BAR();
        STAMP(24);
      }
    });
  }
  {
    const int lane = fresh_lane(), v48 = lane < 48 ? vl16 : OOB;
    auto inbox = [&](int p, int n) { return n < 5 ? XB_OFF + p * 46080 + n * 9216 : AB_OFF + p * 18432 + (n - 5) * 9216; };
    PIN(g3);
    _Pragma("unroll 1") for (int it = 0; it < SPIN_CAP; ++it) {
      const unsigned tl = (unsigned)tag;
      if (__builtin_amdgcn_ballot_w64((((g3.x ^ tl) | (g3.y ^ tl)) | ((g3.z ^ tl) | (g3.w ^ tl))) != 0) == 0) break;
      __builtin_amdgcn_s_sleep(2);
      refill(g3, vl16, sWs, G3_OFF);
      asm volatile("s_waitcnt vmcnt(0)" : "+v"(g3));
    }
    const int vq = (lane >> 4) * 64 + (lane & 3) * 16;
    LAS char* keep = L + w * 768;
#pragma unroll
    for (int h = 0; h < 2; ++h) {
      const int p = 4 * h + ((lane >> 2) & 3), n = (sl - p - 1) & 7, vo = n < 7 ? inbox(rt * 8 + p, n) + vq : OOB;
      const u32x4 o0 = odd8(am[2 * h][0], am[2 * h][1]), o1 = odd8(am[2 * h + 1][0], am[2 * h + 1][1]), o2 = odd8(am[2 * h][2], am[2 * h + 1][2]);
      cst128(o0, rWs, vo, w * 768);
      cst128(o1, rWs, vo, w * 768 + 256);
      cst128(o2, rWs, vo, w * 768 + 512);
      if (n == 7) {
        *(LAS u32x4*)(keep + vq) = o0;
        *(LAS u32x4*)(keep + vq + 256) = o1;
        *(LAS u32x4*)(keep + vq + 512) = o2;
      }
    }
    STAMP(26);
    BAR();
    int flo, fhi;
    bool alo, ahi;
    fin_map(lane, flo, fhi, alo, ahi);
    const u32x4 x2 = *(LAS u32x4*)(L + L_X2 + w * 768 + vl16);
    const f32x4 gl = *(LAS f32x4*)(L + L_GB + flo * 4), gh = *(LAS f32x4*)(L + L_GB + fhi * 4);
    u32x4 p[8], plo, phi;
    p[7] = *(LAS u32x4*)(keep + (lane < 48 ? vl16 : 0));
    pickers(plo, phi);
    _Pragma("unroll 1") for (int it = 0; it < SPIN_CAP; ++it) {
#pragma unroll
      for (int k = 0; k < 7; ++k) fill<1>(p[k], v48, sWs, inbox(pc, k) + w * 768);
      asm volatile("s_waitcnt vmcnt(0)" : "+v"(p[0]), "+v"(p[1]), "+v"(p[2]), "+v"(p[3]), "+v"(p[4]), "+v"(p[5]), "+v"(p[6]));
      u32x4 m = p[0];
#pragma unroll
      for (int k = 1; k < 7; ++k) m &= p[k];
      if (__builtin_amdgcn_ballot_w64(lane < 48 && (m.x & m.y & m.z & m.w & 1u) == 0) == 0) break;
      __builtin_amdgcn_s_sleep(2);
    }
    STAMP(27);
    f32x4 sl4, sh4, sl5, sh5;
    SCHED();
#pragma unroll
    for (int k = 0; k < 8; k += 2) {
      mac(sl4, plo, p[k], k == 0);
      mac(sh4, phi, p[k], k == 0);
      mac(sl5, plo, p[k + 1], k == 0);
      mac(sh5, phi, p[k + 1], k == 0);
    }
    MFMA_DONE();
    sl4 += sl5;
    sh4 += sh5;
    const rsrc_t rO = mkrs(out, S * D * 2);
    const int olo = lane < 48 ? (16 * alo + (lane & 3)) * 1152 + flo * 2 : OOB, ohi = lane < 48 ? (16 * ahi + (lane & 3)) * 1152 + fhi * 2 : OOB;
    __builtin_amdgcn_raw_buffer_store_b64(pk4(up4(x2.x, x2.y) + gl * sl4), rO, olo, row0 * 1152, OST);
    __builtin_amdgcn_raw_buffer_store_b64(pk4(up4(x2.z, x2.w) + gh * sh4), rO, ohi, row0 * 1152, OST);
  }
  asm volatile(".Lfb_hi:");
  STAMP(28);
}

extern "C" void kernel_launch(void* const* d_in, const int* in_sizes, int n_in, void* d_out, int out_size, void* d_ws, size_t ws_size,
                              hipStream_t stream) {
  (void)hipFuncSetAttribute((const void*)fused_block, hipFuncAttributeMaxDynamicSharedMemorySize, LDS_BYTES);
  fused_block<<<256, 768, LDS_BYTES, stream>>>((const u16*)d_in[0], (const u16*)d_in[1], (const u16*)d_in[2], (const u16*)d_in[3],
                                               (const u16*)d_in[4], (const u16*)d_in[6], (const u16*)d_in[7], (const u16*)d_in[8],
                                               (const u16*)d_in[9], (char*)d_ws, (u16*)d_out);
}
```

```cpp
#include <hip/hip_runtime.h>
#include <hip/hip_bf16.h>
#include <stdint.h>
#include <utility>

typedef unsigned short u16;
typedef unsigned long long u64;
typedef __attribute__((ext_vector_type(2))) float f32x2;
typedef __attribute__((ext_vector_type(4))) float f32x4;
typedef __attribute__((ext_vector_type(2))) __bf16 bf16x2;
typedef __attribute__((ext_vector_type(4))) short s16x4;
typedef __attribute__((ext_vector_type(2))) unsigned u32x2;
typedef __attribute__((ext_vector_type(3))) unsigned u32x3;
typedef __attribute__((ext_vector_type(4))) unsigned u32x4;
typedef __amdgpu_buffer_rsrc_t rsrc_t;
#define LAS __attribute__((address_space(3)))
#define LAG __attribute__((address_space(1)))
typedef LAG u64 gu64;

constexpr int S = 2048, D = 576, F = 4608;
constexpr unsigned E_OFF = 0, G0_OFF = 0x1000, G1_OFF = 0x2000, GW_OFF = 0x3000, G3_OFF = 0x5000;
constexpr unsigned MG_OFF = 0x8000;
constexpr unsigned MV_OFF = 0x2c000;
constexpr unsigned AB_OFF = 0x30000;
constexpr unsigned XB_OFF = AB_OFF + 256 * 18432;
constexpr unsigned WT_OFF = XB_OFF + 256 * 46080;
constexpr unsigned WS_END = WT_OFF + 96 * 110592;
constexpr int SC1 = 16;
constexpr int OOB = (int)0x80000000u;
constexpr int L_H = 65536, L_SA = 139264, L_MP = 143872, L_PR = 144384;
constexpr int L_H1 = 73728, L_MV = 140544, L_GB = 147456, L_X2 = 149760, L_CT = 158976, L_ST = 162304;
constexpr int C_MP = L_CT + 16, C_PR = L_CT + 20, C_MV = L_CT + 28, C_Q = L_CT + 32, C_R = L_CT + 48, C_WD = L_CT + 60, C_X = L_CT + 64;
constexpr int LDS_BYTES = 163840;
constexpr int SPIN_CAP = 1 << 16;

template <int N>
using ic = std::integral_constant<int, N>;
template <class Fn, int... I>
__device__ __forceinline__ void sfor_(Fn&& f, std::integer_sequence<int, I...>) { (f(std::integral_constant<int, I>{}), ...); }
template <int N, class Fn>
__device__ __forceinline__ void sfor(Fn&& f) { sfor_(f, std::make_integer_sequence<int, N>{}); }

__device__ __forceinline__ rsrc_t mkrs(const void* p, unsigned bytes) { return __builtin_amdgcn_make_buffer_rsrc((void*)p, 0, bytes, 0x00020000); }
__device__ __forceinline__ u32x4 mksrd(const void* p, unsigned bytes) {
  const u64 a = (u64)p;
  return u32x4{(unsigned)a, (unsigned)(a >> 32) & 0xffffu, bytes, 0x00020000u};
}
#define SCHED() __builtin_amdgcn_sched_barrier(0)
__device__ __forceinline__ void cst128(u32x4 d, rsrc_t r, int v, int s) {
  __builtin_amdgcn_raw_buffer_store_b128(d, r, v, s, SC1);
  SCHED();
  asm volatile("s_nop 1" ::: "memory");
  SCHED();
}
template <int P>
__device__ __forceinline__ void dma(unsigned lds, int voff, u32x4 srd, int soff) {
  if (P == 2) asm volatile("s_mov_b32 m0, %0\n\ts_nop 3\n\tbuffer_load_dwordx4 %1, %2, %3 offen sc1 lds" ::"s"(lds), "v"(voff), "s"(srd), "s"(soff) : "memory");
  else if (P == 1) asm volatile("s_mov_b32 m0, %0\n\ts_nop 3\n\tbuffer_load_dwordx4 %1, %2, %3 offen nt lds" ::"s"(lds), "v"(voff), "s"(srd), "s"(soff) : "memory");
  else asm volatile("s_mov_b32 m0, %0\n\ts_nop 3\n\tbuffer_load_dwordx4 %1, %2, %3 offen lds" ::"s"(lds), "v"(voff), "s"(srd), "s"(soff) : "memory");
}
template <int P>
__device__ __forceinline__ void fill(u32x4& r, int voff, u32x4 srd, int soff) {
  if (P == 1) asm volatile("buffer_load_dwordx4 %0, %1, %2, %3 offen sc1" : "=v"(r) : "v"(voff), "s"(srd), "s"(soff) : "memory");
  else if (P == 2) asm volatile("buffer_load_dwordx4 %0, %1, %2, %3 offen nt" : "=v"(r) : "v"(voff), "s"(srd), "s"(soff) : "memory");
  else asm volatile("buffer_load_dwordx4 %0, %1, %2, %3 offen" : "=v"(r) : "v"(voff), "s"(srd), "s"(soff) : "memory");
}
__device__ __forceinline__ void refill(u32x4& r, int voff, u32x4 srd, int soff) {
  asm volatile("buffer_load_dwordx4 %0, %1, %2, %3 offen sc1" : "+v"(r) : "v"(voff), "s"(srd), "s"(soff) : "memory");
}
template <int P>
__device__ __forceinline__ void fill64(u32x2& r, int voff, u32x4 srd, int soff) {
  if (P == 1) asm volatile("buffer_load_dwordx2 %0, %1, %2, %3 offen sc1" : "=v"(r) : "v"(voff), "s"(srd), "s"(soff) : "memory");
  else asm volatile("buffer_load_dwordx2 %0, %1, %2, %3 offen" : "=v"(r) : "v"(voff), "s"(srd), "s"(soff) : "memory");
}
#define BAR() asm volatile("s_waitcnt lgkmcnt(0)\n\ts_barrier" ::: "memory")
#define VMW(n) asm volatile("s_waitcnt vmcnt(%0)" ::"i"(n) : "memory")
#define VMW_SEL(n, d, lim, ...) \
  asm volatile("s_waitcnt vmcnt(%[hi])\n\ts_cmp_lt_u32 %[ww], %[li]\n\ts_cbranch_scc1 1f\n\ts_waitcnt vmcnt(%[lo])\n1:" \
               : __VA_ARGS__ : [lo] "i"(n), [hi] "i"((n) + (d)), [li] "i"(lim), [ww] "s"(w) : "scc", "memory")
#define PIN(x) asm volatile("" : "+v"(x))
__device__ __forceinline__ int fresh_lane() {
  int l;
  asm volatile("v_mbcnt_lo_u32_b32 %0, -1, 0\n\tv_mbcnt_hi_u32_b32 %0, -1, %0" : "=v"(l));
  return l;
}
__device__ __forceinline__ float bflo(unsigned u) { return __uint_as_float(u << 16); }
__device__ __forceinline__ float bfhi(unsigned u) { return __uint_as_float(u & 0xffff0000u); }
__device__ __forceinline__ unsigned pk(float a, float b) {
  f32x2 v = {a, b};
  return __builtin_bit_cast(unsigned, __builtin_convertvector(v, bf16x2));
}
__device__ __forceinline__ float rbf(float a) { return bflo(pk(a, 0.f)); }
__device__ __forceinline__ float dot2(unsigned a, unsigned b, float c) {
  return __builtin_amdgcn_fdot2_f32_bf16(__builtin_bit_cast(bf16x2, a), __builtin_bit_cast(bf16x2, b), c, false);
}
__device__ __forceinline__ void axpy8(f32x4& a0, f32x4& a1, u32x4 v, unsigned lo, unsigned hi) {
  a0 = f32x4{dot2(v.x, lo, a0[0]), dot2(v.x, hi, a0[1]), dot2(v.y, lo, a0[2]), dot2(v.y, hi, a0[3])};
  a1 = f32x4{dot2(v.z, lo, a1[0]), dot2(v.z, hi, a1[1]), dot2(v.w, lo, a1[2]), dot2(v.w, hi, a1[3])};
}
__device__ __forceinline__ u32x2 pk4(f32x4 a) { return u32x2{pk(a[0], a[1]), pk(a[2], a[3])}; }
__device__ __forceinline__ u32x4 pk8(f32x4 a, f32x4 b) { return u32x4{pk(a[0], a[1]), pk(a[2], a[3]), pk(b[0], b[1]), pk(b[2], b[3])}; }
__device__ __forceinline__ f32x4 up4(unsigned a, unsigned b) { return f32x4{bflo(a), bfhi(a), bflo(b), bfhi(b)}; }
__device__ __forceinline__ f32x4 relu4(f32x4 a) { return f32x4{fmaxf(a[0], 0.f), fmaxf(a[1], 0.f), fmaxf(a[2], 0.f), fmaxf(a[3], 0.f)}; }
__device__ __forceinline__ float nudge(float a) { return __uint_as_float(__float_as_uint(a) + 0x8000u); }
__device__ __forceinline__ u32x4 even8(f32x4 a, f32x4 b) {
  return u32x4{pk(nudge(a[0]), a[1]), pk(nudge(a[2]), a[3]), pk(nudge(b[0]), b[1]), pk(nudge(b[2]), b[3])} & 0xfffffffeu;
}
__device__ __forceinline__ unsigned odd2(float a, float b) { return __builtin_amdgcn_perm(pk(a, b), __float_as_uint(a), 0x07060302u) | 1u; }
__device__ __forceinline__ u32x4 odd8(f32x4 a, f32x4 b) { return u32x4{odd2(a[0], a[1]), odd2(a[2], a[3]), odd2(b[0], b[1]), odd2(b[2], b[3])}; }
__device__ __forceinline__ u32x4 blank() {
  u32x4 o;
  asm volatile("v_mov_b32 %0, -1" : "=v"(o.x));
  asm volatile("v_mov_b32 %0, -1" : "=v"(o.y));
  asm volatile("v_mov_b32 %0, -1" : "=v"(o.z));
  asm volatile("v_mov_b32 %0, -1" : "=v"(o.w));
  return o;
}
__device__ __forceinline__ unsigned top(unsigned m, u32x4 v) { return max(max(m, max(v.x, v.y)), max(v.z, v.w)); }
__device__ __forceinline__ bool absent(unsigned m) { return __builtin_amdgcn_ballot_w64(m == 0xffffffffu) != 0; }
__device__ __forceinline__ void mac(f32x4& c, u32x4 a, u32x4 b, bool first) {
  if (first) asm("v_mfma_f32_16x16x32_bf16 %0, %1, %2, 0" : "=&v"(c) : "v"(a), "v"(b));
  else asm("v_mfma_f32_16x16x32_bf16 %0, %1, %2, %0" : "+v"(c) : "v"(a), "v"(b));
}
__device__ __forceinline__ void pickers(u32x4& lo, u32x4& hi) {
  const int l = fresh_lane();
  const unsigned v = (l >> 4) == ((l >> 2) & 3) ? 0x3f80u << ((l & 1) * 16) : 0u, d0 = (l & 2) ? 0u : v, d1 = (l & 2) ? v : 0u;
  lo = u32x4{d0, d1, 0u, 0u};
  hi = u32x4{0u, 0u, d0, d1};
  PIN(lo);
  PIN(hi);
  SCHED();
  asm volatile("s_nop 1");
  SCHED();
}
#define MFMA_DONE() do { SCHED(); asm volatile("s_nop 7"); SCHED(); } while (0)
template <int C>
__device__ __forceinline__ float dpp(float v) { return __uint_as_float(__builtin_amdgcn_update_dpp(0, __float_as_uint(v), C, 0xf, 0xf, false)); }
template <int C>
__device__ __forceinline__ float dppadd(float v) { return v + dpp<C>(v); }
__device__ __forceinline__ float swap16add(float a, float b) {
  auto r = __builtin_amdgcn_permlane16_swap(__float_as_uint(a), __float_as_uint(b), false, false);
  return __uint_as_float(r[0]) + __uint_as_float(r[1]);
}
__device__ __forceinline__ float swap32add(float a, float b) {
  auto r = __builtin_amdgcn_permlane32_swap(__float_as_uint(a), __float_as_uint(b), false, false);
  return __uint_as_float(r[0]) + __uint_as_float(r[1]);
}
__device__ __forceinline__ float sum16(float v) {
  v = dppadd<0x128>(dppadd<0x124>(v));
  v = swap16add(v, v);
  return swap32add(v, v);
}
__device__ __forceinline__ float colsums(f32x4 a, f32x4 b, bool hi8) {
  const float t0 = swap32add(a[0], b[0]), t1 = swap32add(a[1], b[1]), t2 = swap32add(a[2], b[2]), t3 = swap32add(a[3], b[3]);
  const float u0 = swap16add(t0, t2), u1 = swap16add(t1, t3);
  return (hi8 ? u1 : u0) + dpp<0x128>(hi8 ? u0 : u1);
}
__device__ __forceinline__ float rowsums(const float (&s)[9], bool hi8) {
  const float z = colsums(f32x4{s[0], s[1], s[2], s[3]}, f32x4{s[4], s[5], s[6], s[7]}, hi8) + s[8];
  return dppadd<0x141>(dppadd<0x4e>(dppadd<0xb1>(z)));
}
__device__ __forceinline__ void fst(char* p, u64 v) { __hip_atomic_store((gu64*)p, v, __ATOMIC_RELAXED, __HIP_MEMORY_SCOPE_AGENT); }
__device__ __forceinline__ void lds_wait(LAS char* p, unsigned v) {
  _Pragma("unroll 1") for (int it = 0; it < SPIN_CAP * 16; ++it) {
    if (__hip_atomic_load((LAS unsigned*)p, __ATOMIC_RELAXED, __HIP_MEMORY_SCOPE_WORKGROUP) == v) break;
    __builtin_amdgcn_s_sleep(1);
  }
  asm volatile("" ::: "memory");
}
__device__ __forceinline__ unsigned lds_add(LAS char* p) {
  asm volatile("s_waitcnt lgkmcnt(0)" ::: "memory");
  return __hip_atomic_fetch_add((LAS unsigned*)p, 1u, __ATOMIC_RELAXED, __HIP_MEMORY_SCOPE_WORKGROUP);
}
__device__ __forceinline__ void owner(int f, int& w, int& j) {
  w = (f * 43) >> 7;
  const int r = f - 3 * w;
  j = (w & 1) ? (r == 0 ? 2 : r - 1) : r;
}
constexpr int due(int j) { return (j * 95) / 89; }
constexpr int issued(int it) {
  int n = 0;
  for (int j = 0; j < 90; ++j) n += due(j) < it;
  return n;
}
constexpr int X_W2 = 5, X_OP = 80;
constexpr int extra(int it) { return (it == X_W2) + (it == X_OP); }
constexpr int younger(int it) {
  int n = issued(it) - (it - 18) - 1;
  for (int s = due(it - 18); s < it; ++s) n += extra(s);
  return n;
}

__global__ __launch_bounds__(768, 3) void fused_block(const u16* __restrict__ x, const u16* __restrict__ wre, const u16* __restrict__ wim,
                                                      const u16* __restrict__ dre, const u16* __restrict__ dim, const u16* __restrict__ w1,
                                                      const u16* __restrict__ w2, const u16* __restrict__ cvec, const u16* __restrict__ wmod,
                                                      char* __restrict__ ws, u16* __restrict__ out) {
  extern __shared__ __attribute__((aligned(16))) char smem[];
  LAS char* L = (LAS char*)smem;
  const unsigned lb = (unsigned)(size_t)L;
  const int w = __builtin_amdgcn_readfirstlane(threadIdx.x >> 6);
  const int b = blockIdx.x, x8 = b & 7, idx = b >> 3, g = idx & 3, j8 = idx >> 2;
  const int mat = x8 >> 2, rta = 8 * (x8 & 3) + ((j8 + (((2 * x8 - g) & 3) == 3 ? 2 : 6)) & 7);
  const int kq4 = x8 >> 1, kh = kq4 & 1, R = 8 * (x8 & 1) + j8;
  const int rt = 2 * R + (g >> 1), sl = 2 * kq4 + (g & 1), pc = rt * 8 + sl;
  const int si = rt & 15;
  const int kq = w & 3, cg = w >> 2, crot = g;
  const bool job = idx < 27;
  const rsrc_t rWs = mkrs(ws, WS_END);
  const u32x4 sWs = mksrd(ws, WS_END), sS = mksrd(mat ? wim : wre, S * S * 2);
  const int abown = AB_OFF + ((mat * 4 + g) * 32 + rta) * 18432;
  if (threadIdx.x < 4) *(LAS u32x4*)(L + C_MP + threadIdx.x * 16) = u32x4{0u, 0u, 0u, 0u};
  auto ws_dma = [&](auto NT, int c, int n, int vdma) {
    dma<NT>(lb + (c & 1) * 65536 + n * 1024, vdma, sS, (128 * R + 8 * ((n & 15) ^ ((n >> 4) << 2))) * 4096 + kh * 2048 + ((c + crot) & 3) * 512 + (n >> 4) * 128);
  };
  auto mk_vdma = [&]() {
    const int lane = fresh_lane();
    return (lane >> 3) * 4096 + (((lane & 7) ^ ((4 * (w & 1) + (lane >> 4)) & 7)) * 16);
  };
  u64 tag;
  auto get_tag = [&]() {
    const u32x2 t = *(LAS u32x2*)(L + L_CT);
    tag = ((u64)(unsigned)__builtin_amdgcn_readfirstlane(t.y) << 32) | (unsigned)__builtin_amdgcn_readfirstlane(t.x);
  };
  auto await = [&](u32x2& v, int voff) {
    int n = SPIN_CAP;
    asm volatile(
        "1:\n\tv_cmp_ne_u64 vcc, %[tg], %[v]\n\ts_cbranch_vccz 2f\n\ts_sleep 2\n\tbuffer_load_dwordx2 %[v], %[vo], %[rs], 0 offen sc1\n\t"
        "s_sub_u32 %[n], %[n], 1\n\ts_waitcnt vmcnt(0)\n\ts_cbranch_scc0 1b\n2:"
        : [v] "+v"(v), [n] "+s"(n)
        : [tg] "s"(tag), [vo] "v"(voff), [rs] "s"(sWs)
        : "vcc", "scc", "memory");
  };
  u32x4 V[9], Wd[18];
  const int jm = x8 * 5 + idx - 27;
  const bool urgent = b < 36, mjob = job ? urgent : jm < 36;
  const u32x4 sC = mksrd(cvec, D * 2), sD = mksrd(mat ? dim : dre, D * D * 2);
  const int wr = (w + 2 * g) & 7;
  auto wd_dma = [&](int fl, int at, int q0, int dq, auto NQ) __attribute__((always_inline)) {
    constexpr int nq = NQ;
    const int lane = fresh_lane(), f = 9 * g + fl;
    const int vo = (8 * (lane >> 5) + ((lane >> 3) & 3)) * 1152 + (((lane & 7) ^ (lane >> 4)) * 16);
    const int so = (32 * (f >> 1) + 4 * (f & 1)) * 1152 + q0 * 128;
#pragma unroll
    for (int i = 0; i < nq; ++i) {
      dma<0>(lb + at + i * dq * 2048, vo, sD, so + i * dq * 128);
      dma<0>(lb + at + i * dq * 2048 + 1024, vo ^ 64, sD, so + i * dq * 128 + 16 * 1152);
    }
  };
  auto wd_get = [&](auto K0, auto N) __attribute__((always_inline)) {
    constexpr int k0 = K0, n = N;
    const int lane = fresh_lane();
    const int p = (w < 8 ? w * 10240 : (w < 10 ? 81920 : 90112)) + (lane & 15) * 128 + (((lane >> 4) ^ ((lane >> 1) & 7)) * 16);
    const int pe = w >= 10 ? p ^ 64 : p, po = w >= 10 ? p + 2048 : p ^ 64;
#pragma unroll
    for (int i = 0; i < n; ++i) Wd[k0 + i] = *(LAS u32x4*)(L + ((i & 1) ? po : pe) + (i >> 1) * 2048);
#pragma unroll
    for (int i = 0; i < n; ++i) PIN(Wd[k0 + i]);
  };
  int abso;
  {
    int fw, fj;
    owner(9 * g + (w < 8 ? w : 8), fw, fj);
    abso = abown + (fw - 3 * g) * 6144 + fj * 1024;
  }
  auto ab_blank = [&]() __attribute__((always_inline)) {
    const u32x4 ones = blank();
    const int vl = fresh_lane() * 16;
    cst128(ones, rWs, w <= 8 ? vl : OOB, abso);
    cst128(ones, rWs, w < 8 || w == 9 ? vl : OOB, abso + 3072);
  };
  u32x4 Wr[4];
  u32x2 cv2;
  const int bn = idx * 12 + w, bph = bn >= 162, bkb = ((bn - 162 * bph) * 57) >> 9, bnb = bn - 162 * bph - 9 * bkb;
  const int pitch = job ? (bph ? 1152 : F * 2) : 6912, step = job ? 8 * pitch : pitch, vrow = job ? pitch : 4 * 6912;
  const int wso = job ? (32 * bkb + (bph ? x8 * 576 : 0)) * pitch + (bph ? 0 : x8 * 1152) + bnb * 128 : (288 + 32 * w) * 6912 + 2304 + jm * 128;
  const u32x4 sW = mksrd(job ? (bph ? w2 : w1) : wmod, job ? D * F * 2 : (jm < 36 && w < 9 ? D * 6 * D * 2 : 0u));
  auto w_load = [&]() __attribute__((always_inline)) {
    const int lane = fresh_lane();
    const int vo = (lane >> 3) * vrow + (lane & 7) * 16;
#pragma unroll
    for (int i = 0; i < 4; ++i) fill<2>(Wr[i], vo, sW, wso + i * step);
    fill64<0>(cv2, w < 9 ? (lane >> 3) * 8 : OOB, sC, 576 + 64 * w);
  };
#define V_TIE "+v"(V[0]), "+v"(V[1]), "+v"(V[2]), "+v"(V[3]), "+v"(V[4]), "+v"(V[5]), "+v"(V[6]), "+v"(V[7]), "+v"(V[8])
  auto silu = [&](float c) { return pk(c * __builtin_amdgcn_rcpf(1.f + __expf(-c)), 0.f); };
  unsigned wm;
  __builtin_amdgcn_s_setprio(2);
  if (w >= 4 || !urgent) BAR();
  if (w >= 4) {
    const int lane = fresh_lane();
    {
      const u32x4 sX = mksrd(x, S * D * 2);
      const int so = (64 * rta + 8 * wr) * 1152;
#pragma unroll
      for (int r = 0; r < 8; ++r) fill<1>(V[r], lane * 16, sX, so + r * 1152);
      fill<1>(V[8], (lane >> 3) * 1152 + 1024 + (lane & 7) * 16, sX, so);
    }
    if (lane == 0) lds_add(L + C_X);
    __builtin_amdgcn_s_setprio(0);
    ab_blank();
    asm volatile("s_waitcnt vmcnt(2)" : V_TIE);
    float s1[9];
    unsigned lo = 0x3f80u, hi = 0x3f800000u;
    PIN(lo);
    PIN(hi);
#pragma unroll
    for (int r = 0; r < 9; ++r) s1[r] = dot2(V[r].w, 0x3f803f80u, dot2(V[r].z, 0x3f803f80u, dot2(V[r].y, 0x3f803f80u, dot2(V[r].x, 0x3f803f80u, 0.f))));
    float nm = rowsums(s1, lane & 8) * (-1.f / D);
    PIN(nm);
#pragma unroll
    for (int r = 0; r < 9; ++r) {
      float e[8];
#define CENTRE(CON, m)                                                                                                 \
  asm volatile(                                                                                                        \
      "s_nop 1\n\tv_dot2_f32_bf16 %0, %8, %12, %14\n\tv_dot2_f32_bf16 %1, %8, %13, %14\n\tv_dot2_f32_bf16 %2, %9, %12, %14\n\t" \
      "v_dot2_f32_bf16 %3, %9, %13, %14\n\tv_dot2_f32_bf16 %4, %10, %12, %14\n\tv_dot2_f32_bf16 %5, %10, %13, %14\n\t" \
      "v_dot2_f32_bf16 %6, %11, %12, %14\n\tv_dot2_f32_bf16 %7, %11, %13, %14\n\ts_nop 2"                              \
      : "=&v"(e[0]), "=&v"(e[1]), "=&v"(e[2]), "=&v"(e[3]), "=&v"(e[4]), "=&v"(e[5]), "=&v"(e[6]), "=&v"(e[7])         \
      : "v"(V[r].x), "v"(V[r].y), "v"(V[r].z), "v"(V[r].w), "v"(lo), "v"(hi), CON(m))
      if (r < 8) CENTRE("s", (__builtin_amdgcn_readlane(__float_as_uint(nm), 8 * (r & 7))));
      else CENTRE("v", (nm));
      const f32x2 d0 = {e[0], e[1]}, d1 = {e[2], e[3]}, d2 = {e[4], e[5]}, d3 = {e[6], e[7]};
      const f32x2 t = (d0 * d0 + d1 * d1) + (d2 * d2 + d3 * d3);
      s1[r] = t.x + t.y;
    }
    float rs = rsqrtf(rowsums(s1, lane & 8) * (1.f / D) + 1e-5f), sh = nm * rs;
    PIN(rs);
    PIN(sh);
    lds_wait(L + C_WD, 4);
    wd_get(ic<0>{}, ic<10>{});
    if (w < 8) wd_dma(w, w * 10240, 5, 1, ic<4>{});
#pragma unroll
    for (int r = 0; r < 9; ++r) {
      const float a = r < 8 ? __uint_as_float(__builtin_amdgcn_readlane(__float_as_uint(rs), 8 * (r & 7))) : rs;
      const float c = r < 8 ? __uint_as_float(__builtin_amdgcn_readlane(__float_as_uint(sh), 8 * (r & 7))) : sh;
      V[r] = pk8(up4(V[r].x, V[r].y) * a + c, up4(V[r].z, V[r].w) * a + c);
      PIN(V[r]);
    }
    VMW(0);
  } else {
    const int lane = fresh_lane(), rb = (job ? 288 * (b & 1) : 0) + 72 * w;
    u32x2 ev;
    fill64<1>(ev, 0, sWs, E_OFF);
    {
      u64 pc0;
      unsigned len;
      asm volatile("s_getpc_b64 %0\n.Lfb_lo:\n\ts_mov_b32 %1, .Lfb_hi-.Lfb_lo" : "=s"(pc0), "=s"(len));
      const u32x4 sI = mksrd(w ? (const void*)(mat ? dim : dre) : (const void*)pc0, w ? D * D * 2 : len + 0u);
      asm volatile("buffer_load_dword %0, %1, %2, %3 offen sc1"
                   : "=v"(wm)
                   : "v"((w ? idx >= 5 : lane < 16) ? lane * 128 : OOB), "s"(sI), "s"(w ? ((idx - 5) * 3 + w - 1) * 8192 : idx * 2048)
                   : "memory");
    }
    auto set_tag = [&]() {
      tag = (((u64)(unsigned)__builtin_amdgcn_readfirstlane(ev.y) << 32) | (unsigned)__builtin_amdgcn_readfirstlane(ev.x)) + 1;
    };
    if (mjob) {
      const u32x4 sM = mksrd(wmod, D * 6 * D * 2);
      const int so = rb * 6912 + (job ? (b >> 1) * 128 : 2304 + jm * 128);
      u32x4 cw;
      unsigned cx, sv[9];
      fill<0>(cw, (lane >> 3) * 16, sC, rb * 2);
      asm volatile("buffer_load_ushort %0, %1, %2, %3 offen" : "=v"(cx) : "v"((lane >> 3) * 2), "s"(sC), "s"(rb * 2 + 128) : "memory");
#pragma unroll
      for (int i = 0; i < 8; ++i) fill<2>(V[i], (lane >> 3) * 8 * 6912 + (lane & 7) * 16, sM, so + i * 6912);
      fill<2>(V[8], (lane >> 3) * 6912 + (lane & 7) * 16, sM, so + 64 * 6912);
      if (urgent) BAR();
      asm volatile("s_waitcnt vmcnt(9)" : "+v"(cw), "+v"(cx), "+v"(ev));
      set_tag();
#pragma unroll
      for (int i = 0; i < 9; ++i) {
        const unsigned u = i < 8 ? cw[i >> 1] : cx;
        sv[i] = silu((i & 1) ? bfhi(u) : bflo(u));
        PIN(sv[i]);
      }
      asm volatile("s_waitcnt vmcnt(0)" : V_TIE);
      f32x4 a0 = {0.f, 0.f, 0.f, 0.f}, a1 = a0;
#pragma unroll
      for (int i = 0; i < 9; ++i) axpy8(a0, a1, V[i], sv[i], sv[i] << 16);
      *(LAS float*)(L + L_MP + w * 256 + (lane & 7) * 32 + (lane >> 3) * 4) = colsums(a0, a1, lane & 8);
      unsigned n = 0;
      if (lane == 0) n = lds_add(L + C_MP);
      if (__builtin_amdgcn_readfirstlane(n) == 3) {
        asm volatile("" ::: "memory");
        LAS char* p = L + L_MP + (lane & 31) * 8;
        const f32x2 v = (*(LAS f32x2*)p + *(LAS f32x2*)(p + 256)) + (*(LAS f32x2*)(p + 512) + *(LAS f32x2*)(p + 768));
        if (job) {
#pragma unroll
          for (int i = 0; i < 4; ++i)
            cst128(u32x4{__float_as_uint(v.x), (unsigned)tag, __float_as_uint(v.y), (unsigned)tag}, rWs, (lane >> 5) * 18432 + (lane & 31) * 16,
                   MG_OFF + i * 36864 + (b & 1) * 9216 + (b >> 1) * 512);
        } else {
          *(LAS f32x2*)p = v;
        }
      }
    }
    __builtin_amdgcn_s_setprio(0);
    ab_blank();
    lds_wait(L + C_X, 8);
    wd_dma(w + 4, (w + 4) * 10240, 0, 1, ic<5>{});
    wd_dma(8, 81920 + w * 2048, w, 4, ic<2>{});
    if (w == 0) wd_dma(8, 81920 + 16384, 8, 1, ic<1>{});
    wd_dma(w, w * 10240, 0, 1, ic<5>{});
    asm volatile("s_waitcnt vmcnt(0)" : "+v"(ev), "+v"(wm));
    if (lane == 0) lds_add(L + C_WD);
    set_tag();
    const unsigned tl = (unsigned)tag;
    if (w == 0 && lane == 0) *(LAS u64*)(L + L_CT) = tag;
    wd_get(ic<0>{}, ic<10>{});
    wd_dma(w, w * 10240, 5, 1, ic<4>{});
    const int mo = MG_OFF + x8 * 18432 + w * 1024, vl = lane * 16, v2 = w == 0 ? vl : OOB;
    u32x4 p[6];
    _Pragma("unroll 1") for (int it = 0; it < SPIN_CAP; ++it) {
#pragma unroll
      for (int i = 0; i < 6; ++i) fill<1>(p[i], i < 4 ? vl : v2, sWs, mo + (i >> 1) * 4096 + (i & 1) * 9216);
      asm volatile("s_waitcnt vmcnt(0)" : "+v"(p[0]), "+v"(p[1]), "+v"(p[2]), "+v"(p[3]), "+v"(p[4]), "+v"(p[5]));
      unsigned d = 0, e = 0;
#pragma unroll
      for (int i = 0; i < 4; ++i) d |= (p[i].y ^ tl) | (p[i].w ^ tl);
#pragma unroll
      for (int i = 4; i < 6; ++i) e |= (p[i].y ^ tl) | (p[i].w ^ tl);
      if (__builtin_amdgcn_ballot_w64((w == 0 ? d | e : d) != 0) == 0) break;
      __builtin_amdgcn_s_sleep(2);
    }
#pragma unroll
    for (int i = 0; i < 3; ++i) {
      unsigned o = pk(__uint_as_float(p[2 * i].x) + __uint_as_float(p[2 * i + 1].x), __uint_as_float(p[2 * i].z) + __uint_as_float(p[2 * i + 1].z));
      const bool sc = (w + 4 * i) * 64 + lane >= 288;
      if (sc) o = pk(bflo(o) + 1.f, bfhi(o) + 1.f);
      if (i < 2 || w == 0) *(LAS u32x2*)(L + L_SA + (w + 4 * i) * 512 + lane * 8) = u32x2{sc ? o & 0xffffu : o << 16, o & 0xffff0000u};
    }
  }
  w_load();
  if (w < 8) wd_get(ic<10>{}, ic<8>{});
  BAR();
  u32x2 gv;
  if (w >= 4) {
    get_tag();
    const int lane = fresh_lane();
    if (w == 4 && lane == 0) fst(ws + G0_OFF + ((mat * 4 + g) * 32 + rta) * 8, tag);
    LAS char* sp = L + L_SA + lane * 32;
    LAS char* sq = L + L_SA + 2048 + (lane & 7) * 32;
    const int hb = L_H + (wr >> 2) * 2048 + (lane & 3) * 256 + (((4 * wr) ^ (lane & 7)) & 15) * 16;
    auto affine = [&](u32x4 v, f32x4 c0, f32x4 c1, f32x4 s0, f32x4 s1) {
      float e[8];
      asm volatile(
          "v_dot2_f32_bf16 %0, %8, %12, %20\n\tv_dot2_f32_bf16 %1, %8, %13, %21\n\tv_dot2_f32_bf16 %2, %9, %14, %22\n\t"
          "v_dot2_f32_bf16 %3, %9, %15, %23\n\tv_dot2_f32_bf16 %4, %10, %16, %24\n\tv_dot2_f32_bf16 %5, %10, %17, %25\n\t"
          "v_dot2_f32_bf16 %6, %11, %18, %26\n\tv_dot2_f32_bf16 %7, %11, %19, %27\n\ts_nop 2"
          : "=&v"(e[0]), "=&v"(e[1]), "=&v"(e[2]), "=&v"(e[3]), "=&v"(e[4]), "=&v"(e[5]), "=&v"(e[6]), "=&v"(e[7])
          : "v"(v.x), "v"(v.y), "v"(v.z), "v"(v.w), "v"(c0[0]), "v"(c0[1]), "v"(c0[2]), "v"(c0[3]), "v"(c1[0]), "v"(c1[1]), "v"(c1[2]), "v"(c1[3]),
            "v"(s0[0]), "v"(s0[1]), "v"(s0[2]), "v"(s0[3]), "v"(s1[0]), "v"(s1[1]), "v"(s1[2]), "v"(s1[3]));
      return u32x4{pk(e[0], e[1]), pk(e[2], e[3]), pk(e[4], e[5]), pk(e[6], e[7])};
    };
    const f32x4 sa0 = *(LAS f32x4*)sp, sa1 = *(LAS f32x4*)(sp + 16), ca0 = *(LAS f32x4*)(sp + 2304), ca1 = *(LAS f32x4*)(sp + 2320);
#pragma unroll
    for (int r = 0; r < 8; ++r) {
      *(LAS u32x4*)(L + ((hb + (lane >> 2) * 4096) ^ ((r & 3) * 16)) + (r >> 2) * 1024) = affine(V[r], ca0, ca1, sa0, sa1);
      if ((r & 1) && r < 7) SCHED();
    }
    const f32x4 ta0 = *(LAS f32x4*)sq, ta1 = *(LAS f32x4*)(sq + 16), da0 = *(LAS f32x4*)(sq + 2304), da1 = *(LAS f32x4*)(sq + 2320);
    *(LAS u32x4*)(L + ((hb + 65536 + (lane & 4) * 1024 + (lane >> 5) * 1024) ^ (((lane >> 3) & 3) * 16))) = affine(V[8], da0, da1, ta0, ta1);
  } else {
    const int vdma = mk_vdma();
#pragma unroll
    for (int i = 0; i < 8; ++i) ws_dma(ic<0>{}, 0, w + 4 * i, vdma);
  }
  BAR();
  {
    const int lane = fresh_lane();
    const int ra = L_H + (lane >> 4) * 256 + (((lane & 15) ^ (lane >> 4)) * 16);
    const unsigned neg = mat ? 0x80008000u : 0u;
    const int so = abso;
    f32x4 c[4];
    if (w < 8) {
      u32x4 Hf[3][4];
      const int vdma = mk_vdma();
      auto ldh = [&](int kb) {
#pragma unroll
        for (int rb = 0; rb < 4; ++rb) Hf[kb % 3][rb] = *(LAS u32x4*)(L + (ra ^ (kb & 1) * 64) + kb * 4096 + rb * 1024);
      };
#pragma unroll
      for (int i = 0; i < 2; ++i) ldh(i);
      sfor<18>([&](auto KB) {
        constexpr int kb = KB;
        if (kb + 2 < 18) ldh(kb + 2);
        SCHED();
#pragma unroll
        for (int rb = 0; rb < 4; ++rb) mac(c[rb], Hf[kb % 3][rb], Wd[kb], kb == 0);
        SCHED();
        if (kb < 16 && (kb & 1) && w < 4) ws_dma(ic<0>{}, 0, w + 4 * (8 + kb / 2), vdma);
        SCHED();
      });
      MFMA_DONE();
      cst128(even8(c[0], c[1]) ^ neg, rWs, lane * 16, so);
      cst128(even8(c[2], c[3]) ^ neg, rWs, lane * 16, so + 3072);
      VMW_SEL(2, 16, 4, );
    } else {
      const int g0 = G0_OFF + ((mat * 4 + g) * 32 + 16 * kh + (lane & 15)) * 8;
      fill64<1>(gv, w == 11 ? g0 : OOB, sWs, 0);
      const int rk = (ra ^ (w >= 10 ? 64 : 0)) + (w >= 10 ? 9 * 4096 : 0) + (w & 1) * 2048;
      u32x4 Hf[4][2];
      auto ldh = [&](int i) {
        Hf[i & 3][0] = *(LAS u32x4*)(L + (rk ^ (i & 1) * 64) + i * 4096);
        Hf[i & 3][1] = *(LAS u32x4*)(L + (rk ^ (i & 1) * 64) + i * 4096 + 1024);
      };
      ldh(0);
      ldh(1);
      ldh(2);
#pragma unroll
      for (int i = 0; i < 9; ++i) {
        if (i < 6) ldh(i + 3);
        SCHED();
        mac(c[0], Hf[i & 3][0], Wd[i], i == 0);
        mac(c[1], Hf[i & 3][1], Wd[i], i == 0);
        SCHED();
      }
      MFMA_DONE();
      LAS char* pp = L + L_PR + (w & 1) * 2048 + lane * 16;
      if (w >= 10) {
        *(LAS f32x4*)pp = c[0];
        *(LAS f32x4*)(pp + 1024) = c[1];
        if (lane == 0) lds_add(L + C_PR + (w & 1) * 4);
        asm volatile("s_waitcnt vmcnt(0)" : "+v"(gv));
        if (w == 11) await(gv, g0);
      } else {
        lds_wait(L + C_PR + (w & 1) * 4, 1);
        cst128(even8(c[0] + *(LAS f32x4*)pp, c[1] + *(LAS f32x4*)(pp + 1024)) ^ neg, rWs, lane * 16, so + (w & 1) * 3072);
        asm volatile("s_waitcnt vmcnt(1)" : "+v"(gv));
      }
    }
  }
#pragma unroll
  for (int i = 0; i < 4; ++i) PIN(Wr[i]);
  PIN(cv2);
  BAR();
  int vl16, rd0;
  {
    const int lane = fresh_lane();
    vl16 = lane * 16;
    rd0 = kq * 16384 + (lane & 15) * 128 + (((lane >> 4) ^ ((lane & 15) >> 1)) * 16);
    LAS char* sp = L + L_H + w * 4096;
    if (job) {
#pragma unroll
      for (int i = 0; i < 4; ++i) *(LAS u32x4*)(sp + i * 1024 + vl16) = Wr[i];
      LAS char* rp = sp + ((lane >> 4) * 8 + ((lane >> 2) & 3)) * 128 + (lane & 3) * 16;
#pragma unroll
      for (int i = 0; i < 4; ++i) {
        const u32x2 lo = __builtin_bit_cast(u32x2, __builtin_amdgcn_ds_read_tr16_b64_v4i16((LAS s16x4*)(rp + (i >> 1) * 64 + (i & 1) * 8)));
        const u32x2 hi = __builtin_bit_cast(u32x2, __builtin_amdgcn_ds_read_tr16_b64_v4i16((LAS s16x4*)(rp + (i >> 1) * 64 + (i & 1) * 8 + 512)));
        Wr[i] = u32x4{lo.x, lo.y, hi.x, hi.y};
      }
    } else {
      f32x4 a0 = {0.f, 0.f, 0.f, 0.f}, a1 = a0;
#pragma unroll
      for (int i = 0; i < 4; ++i) {
        const unsigned u = cv2[i >> 1], t = silu((i & 1) ? bfhi(u) : bflo(u));
        axpy8(a0, a1, Wr[i], t, t << 16);
      }
      *(LAS float*)(sp + (lane & 7) * 32 + (lane >> 3) * 4) = colsums(a0, a1, lane & 8);
      unsigned n = 0;
      if (lane == 0) n = lds_add(L + C_MV);
      const bool last = __builtin_amdgcn_readfirstlane(n) == 11;
      asm volatile("" ::: "memory");
      f32x4 v = *(LAS f32x4*)(L + L_MP + (lane & 15) * 16);
#pragma unroll
      for (int k = 0; k < 9; ++k) v += *(LAS f32x4*)(L + L_H + k * 4096 + (lane & 15) * 16);
      u32x2 o = pk4(v);
      if (jm >= 18 && jm < 27) o = pk4(up4(o.x, o.y) + 1.f);
      cst128(u32x4{o.x, (unsigned)tag, o.y, (unsigned)tag}, rWs, last && jm < 36 && lane < 16 ? vl16 : OOB, MV_OFF + jm * 256);
    }
  }
  const int abin = AB_OFF + ((mat * 4 + g) * 32 + 16 * kh + kq) * 18432 + cg * 6144;
  auto ab_at = [&](int c) { return abin + ((c + crot) & 3) * 73728; };
  u32x4 Ra[6];
  _Pragma("unroll 1") for (int it = 0; it < SPIN_CAP; ++it) {
#pragma unroll
    for (int i = 0; i < 6; ++i) fill<1>(Ra[i], vl16, sWs, ab_at(0) + i * 1024);
    asm volatile("s_waitcnt vmcnt(0)" : "+v"(Ra[0]), "+v"(Ra[1]), "+v"(Ra[2]), "+v"(Ra[3]), "+v"(Ra[4]), "+v"(Ra[5]));
    unsigned m = 0;
#pragma unroll
    for (int i = 0; i < 6; ++i) m = top(m, Ra[i]);
    if (!absent(m)) break;
  }
#pragma unroll
  for (int i = 0; i < 4; ++i) {
    int fw, fj;
    owner(4 * bnb + i, fw, fj);
    cst128(Wr[i], rWs, job ? vl16 : OOB, WT_OFF + (x8 * 12 + fw) * 110592 + (bph * 54 + bkb * 3 + fj) * 1024);
  }
  BAR();
  f32x4 acc[8][3];
  auto touch = [&](unsigned& r, int ph) {
    const int lane = fresh_lane();
    asm volatile("buffer_load_dword %0, %1, %2, %3 offen sc1"
                 : "=v"(r)
                 : "v"(lane < 27 ? lane * 128 : OOB), "s"(sWs), "s"(WT_OFF + (sl * 12 + w) * 110592 + ph * 55296 + si * 3456)
                 : "memory");
  };
  {
    u32x4 Wf[2][4];
    auto ldw = [&](int s, int h) {
#pragma unroll
      for (int r = 0; r < 4; ++r) Wf[h][r] = *(LAS u32x4*)(L + ((s >> 1) & 1) * 65536 + (4 * h + r) * 2048 + (rd0 ^ ((s & 1) * 64)));
    };
    auto gw_at = [&]() { return GW_OFF + (sl * 32 + (fresh_lane() & 31)) * 8; };
    const int wq = w < 9 ? w : w - 9;
    sfor<8>([&](auto SS) {
      constexpr int s = SS, c = s >> 1, kk = s & 1;
      if constexpr (s >= 2) {
        constexpr int y = s == 7 ? 2 : (kk ? (s == 3 ? 11 : 9) : 3);
        asm volatile("s_waitcnt vmcnt(%3)" : "+v"(Ra[3 * kk]), "+v"(Ra[3 * kk + 1]), "+v"(Ra[3 * kk + 2]) : "i"(y));
      }
      int vdma;
      if constexpr (kk == 0) {
        if constexpr (s > 0) BAR();
        if constexpr (s == 4) {
          u32x4 mv = *(LAS u32x4*)(L + L_X2 + wq * 1024 + vl16);
          ldw(s, 0);
          _Pragma("unroll 1") for (int it = 0; it < SPIN_CAP; ++it) {
            if (__builtin_amdgcn_ballot_w64(((mv.y ^ (unsigned)tag) | (mv.w ^ (unsigned)tag)) != 0) == 0) break;
            refill(mv, vl16, sWs, MV_OFF + wq * 1024);
            asm volatile("s_waitcnt vmcnt(0)" : "+v"(mv));
          }
          *(LAS f32x4*)(L + L_MV + wq * 1024 + vl16) = up4(mv.x, mv.z);
          SCHED();
        } else {
          ldw(s, 0);
        }
        ldw(s, 1);
        SCHED();
        if constexpr (c < 3) vdma = mk_vdma();
        if constexpr (s == 2) {
          if (w == 11 && fresh_lane() == 0) fst(ws + GW_OFF + (x8 * 32 + idx) * 8, tag);
          dma<2>(lb + L_X2 + wq * 1024, vl16, sWs, MV_OFF + wq * 1024);
        }
        if constexpr (s == 4) {
          if (w == 11 && fresh_lane() == 0) fst(ws + G1_OFF + pc * 8, tag);
          fill64<1>(gv, gw_at(), sWs, 0);
        }
        if constexpr (s == 6) {
          PIN(gv);
          await(gv, gw_at());
          touch(wm, 0);
          fill64<1>(gv, G1_OFF + (16 * R + (fresh_lane() & 15)) * 8, sWs, 0);
        }
      }
      if constexpr (s >= 2) {
        if (absent(top(top(top(0u, Ra[3 * kk]), Ra[3 * kk + 1]), Ra[3 * kk + 2]))) {
          _Pragma("unroll 1") for (int it = 0; it < SPIN_CAP; ++it) {
#pragma unroll
            for (int j = 0; j < 3; ++j) refill(Ra[3 * kk + j], vl16, sWs, ab_at(c) + kk * 3072 + j * 1024);
            asm volatile("s_waitcnt vmcnt(0)" : "+v"(Ra[3 * kk]), "+v"(Ra[3 * kk + 1]), "+v"(Ra[3 * kk + 2]));
            if (!absent(top(top(top(0u, Ra[3 * kk]), Ra[3 * kk + 1]), Ra[3 * kk + 2]))) break;
          }
        }
      }
      SCHED();
      sfor<3>([&](auto J) {
        constexpr int j = J;
#pragma unroll
        for (int r = 0; r < 4; ++r) mac(acc[r][j], Ra[3 * kk + j], Wf[0][r], s == 0);
        SCHED();
        if constexpr (kk == 0 && c < 3) ws_dma(ic<0>{}, c + 1, w + 12 * (2 * j), vdma);
        if constexpr (kk == 0 && s < 4 && j < 2)
          cst128(blank(), rWs, s < 2 || j < 1 || w < 9 ? vl16 : OOB, XB_OFF + pc * 46080 + (w + 12 * (s + j)) * 1024);
        if constexpr (kk == 0 && j == 2) ldw(s + 1, 0);
        SCHED();
#pragma unroll
        for (int r = 0; r < 4; ++r) mac(acc[4 + r][j], Ra[3 * kk + j], Wf[1][r], s == 0);
        SCHED();
        if constexpr (kk == 0 && j == 2) ldw(s + 1, 1);
        if constexpr (c < 3) refill(Ra[3 * kk + j], vl16, sWs, ab_at(c + 1) + kk * 3072 + j * 1024);
        if constexpr (kk == 0 && c < 3) {
          if constexpr (j < 2) ws_dma(ic<0>{}, c + 1, w + 12 * (2 * j + 1), vdma);
          else if (w < 4) ws_dma(ic<0>{}, c + 1, 60 + w, vdma);
        }
        SCHED();
      });
    });
  }
  MFMA_DONE();
  __builtin_amdgcn_s_setprio(1);
  auto fin_map = [&](int w, int lane, int& flo, int& fhi, bool& alo, bool& ahi) {
    const int s = lane >> 4, f2 = 3 * w + ((w & 1) ? 0 : 2);
    flo = 8 * ((lane >> 2) & 3) + (s < 2 ? 32 * ((3 * w + 1) >> 1) : 32 * (f2 >> 1) + 4 * (f2 & 1));
    fhi = flo + (s < 2 ? 4 : 0);
    alo = s == 1;
    ahi = s >= 1;
  };
  const int row0 = 32 * (pc >> 2) + 4 * (pc & 3);
  const int rot = (si * 18) >> 4, wb = WT_OFF + (sl * 12 + w) * 110592;
  auto w_at = [&](int it) {
    const int ph = it / 54, k = (it - 54 * ph) / 3 + rot;
    return wb + ph * 55296 + (k >= 18 ? k - 18 : k) * 3072 + (it % 3) * 1024;
  };
  u32x4 Rw[18];
  {
    auto area = [&](int v) { return v < 7 ? v * 9216 : (v == 7 ? 131072 : 65536 + (v - 8) * 16384); };
    u32x4 t[9], plo, phi;
#pragma unroll
    for (int i = 1; i < 4; ++i) {
      t[3 * i - 3] = pk8(acc[2 * i][0], acc[2 * i][1]);
      t[3 * i - 2] = pk8(acc[2 * i + 1][0], acc[2 * i + 1][1]);
      t[3 * i - 1] = pk8(acc[2 * i][2], acc[2 * i + 1][2]);
    }
    if (w >= 8) lds_wait(L + C_Q + kq * 4, 2);
    else if (vl16 == 0) lds_add(L + C_Q + kq * 4);
#pragma unroll
    for (int i = 0; i < 9; ++i) *(LAS u32x4*)(L + area(w) + i * 1024 + vl16) = t[i];
    if (vl16 == 0) lds_add(L + C_R + cg * 4);
    const int lane = fresh_lane(), v48 = lane < 48 ? vl16 : OOB;
    int flo, fhi;
    bool alo, ahi;
    const int cb = 3 * kq + cg;
    fin_map(cb, lane, flo, fhi, alo, ahi);
    u32x2 xa, xb;
    {
      const u32x4 sX = mksrd(x, S * D * 2);
      fill64<0>(xa, lane < 48 ? (16 * alo + (lane & 3)) * 1152 + flo * 2 : OOB, sX, row0 * 1152);
      fill64<0>(xb, lane < 48 ? (16 * ahi + (lane & 3)) * 1152 + fhi * 2 : OOB, sX, row0 * 1152);
    }
    pickers(plo, phi);
    lds_wait(L + C_R + cg * 4, 4);
#pragma unroll
    for (int i = 0; i < 9; ++i) t[i] = *(LAS u32x4*)(L + area(w ^ (i / 3 + 1)) + i * 1024 + vl16);
#pragma unroll
    for (int i = 0; i < 3; ++i) {
      mac(acc[0][0], plo, t[3 * i], false);
      mac(acc[0][1], phi, t[3 * i], false);
      mac(acc[1][0], plo, t[3 * i + 1], false);
      mac(acc[1][1], phi, t[3 * i + 1], false);
      mac(acc[0][2], plo, t[3 * i + 2], false);
      mac(acc[1][2], phi, t[3 * i + 2], false);
    }
    MFMA_DONE();
    asm volatile("s_waitcnt vmcnt(2)" : "+v"(gv), "+v"(wm));
    await(gv, G1_OFF + (16 * R + (lane & 15)) * 8);
    const int vpub = ((lane >> 2) & 3) * 46080 + (lane >> 4) * 64 + (lane & 3) * 16;
    const int yb = XB_OFF + (4 * R + kq) * 4 * 46080 + kq4 * 9216 + (3 * g + cg) * 768;
    cst128(even8(acc[0][0], acc[0][1]), rWs, vpub, yb);
    cst128(even8(acc[1][0], acc[1][1]), rWs, vpub, yb + 256);
    cst128(even8(acc[0][2], acc[1][2]), rWs, vpub, yb + 512);
    u32x4 yp[4];
    __builtin_amdgcn_s_setprio(0);
    auto ldr = [&](int lo, int hi) __attribute__((always_inline)) {
#pragma unroll
      for (int i = 0; i < 18; ++i)
        if (i >= lo && i < hi) refill(Rw[i], vl16, sWs, w_at(i));
    };
#pragma unroll
    for (int i = 0; i < 18; ++i) asm volatile("" : "=v"(Rw[i]));
    if (w < 8) ldr(0, 9);
    else __builtin_amdgcn_s_sleep(8);
#pragma unroll
    for (int p = 0; p < 4; ++p) fill<1>(yp[p], v48, sWs, XB_OFF + pc * 46080 + p * 9216 + cb * 768);
    __builtin_amdgcn_s_setprio(1);
    asm volatile("s_waitcnt vmcnt(0)" : "+v"(yp[0]), "+v"(yp[1]), "+v"(yp[2]), "+v"(yp[3]), "+v"(xa), "+v"(xb), "+v"(wm));
    _Pragma("unroll 1") for (int it = 0; it < SPIN_CAP; ++it) {
      if (!absent(top(top(top(top(0u, yp[0]), yp[1]), yp[2]), yp[3]))) break;
      __builtin_amdgcn_s_sleep(1);
#pragma unroll
      for (int p = 0; p < 4; ++p) refill(yp[p], v48, sWs, XB_OFF + pc * 46080 + p * 9216 + cb * 768);
      asm volatile("s_waitcnt vmcnt(0)" : "+v"(yp[0]), "+v"(yp[1]), "+v"(yp[2]), "+v"(yp[3]));
    }
    f32x4 yl, yh;
    SCHED();
#pragma unroll
    for (int p = 0; p < 4; ++p) {
      mac(yl, plo, yp[p], p == 0);
      mac(yh, phi, yp[p], p == 0);
    }
    SCHED();
    const f32x4 gal = *(LAS f32x4*)(L + L_MV + flo * 4), gah = *(LAS f32x4*)(L + L_MV + fhi * 4);
    const f32x4 sbl = *(LAS f32x4*)(L + L_MV + 2304 + flo * 4), sbh = *(LAS f32x4*)(L + L_MV + 2304 + fhi * 4);
    const f32x4 cbl = *(LAS f32x4*)(L + L_MV + 4608 + flo * 4), cbh = *(LAS f32x4*)(L + L_MV + 4608 + fhi * 4);
    MFMA_DONE();
    const u32x4 x2 = pk8(up4(xa.x, xa.y) + gal * yl, up4(xb.x, xb.y) + gah * yh);
    yl = up4(x2.x, x2.y);
    yh = up4(x2.z, x2.w);
    {
      const bool on = lane < 48;
      const float lo = (yl[0] + yl[1]) + (yl[2] + yl[3]), hi = (yh[0] + yh[1]) + (yh[2] + yh[3]);
      const float ma = sum16((alo ? 0.f : lo) + (ahi ? 0.f : hi)) * (1.f / 48), mb = sum16((alo ? lo : 0.f) + (ahi ? hi : 0.f)) * (1.f / 48);
      const f32x4 dl = yl - (alo ? mb : ma), dh = yh - (ahi ? mb : ma), ql = dl * dl, qh = dh * dh;
      const float q0 = on ? (ql[0] + ql[1]) + (ql[2] + ql[3]) : 0.f, q1 = on ? (qh[0] + qh[1]) + (qh[2] + qh[3]) : 0.f;
      const float qa = sum16((alo ? 0.f : q0) + (ahi ? 0.f : q1)), qb = sum16((alo ? q0 : 0.f) + (ahi ? q1 : 0.f));
      if (lane < 4) *(LAS f32x4*)(L + L_ST + (w * 4 + lane) * 16) = f32x4{ma, mb, qa, qb};
    }
    if (lane < 48) *(LAS u32x4*)(L + L_X2 + cb * 768 + vl16) = x2;
    if (w < 8) ldr(9, 18);
    BAR();
    {
      const f32x4 e = *(LAS f32x4*)(L + L_ST + (lane < 48 ? vl16 : 0));
      const bool on = lane < 48;
      const float ma = sum16(on ? e[0] : 0.f) * (1.f / 12), mb = sum16(on ? e[1] : 0.f) * (1.f / 12);
      const float da = e[0] - ma, db = e[1] - mb;
      const float va = rsqrtf(sum16(on ? e[2] + 48.f * da * da : 0.f) * (1.f / D) + 1e-5f);
      const float vb = rsqrtf(sum16(on ? e[3] + 48.f * db * db : 0.f) * (1.f / D) + 1e-5f);
      cst128(even8((yl - (alo ? mb : ma)) * (alo ? vb : va) * cbl + sbl, (yh - (ahi ? mb : ma)) * (ahi ? vb : va) * cbh + sbh), rWs, v48,
             XB_OFF + pc * 46080 + 36864 + cb * 768);
    }
    if (w == 11 && b == 0 && lane == 0) fst(ws + E_OFF, tag);
    u32x4 h[8];
    __builtin_amdgcn_s_setprio(0);
    if (w >= 8) ldr(0, 18);
    else __builtin_amdgcn_s_sleep(24);
#pragma unroll
    for (int p = 0; p < 8; ++p) fill<1>(h[p], v48, sWs, XB_OFF + (rt * 8 + p) * 46080 + 36864 + cb * 768);
    const int kp = (flo >> 5) - rot, lg = (lane >> 2) & 3;
    const int hl = (kp < 0 ? kp + 18 : kp) * 4096 + lg * 256 + (lg & 1) * 192 + (lane & 3) * 16 + (flo & 4) * 2;
    const int olo = hl + alo * 1024, ohi = hl + ahi * 1024 + (fhi - flo) * 2;
    asm volatile("s_waitcnt vmcnt(0)" : "+v"(h[0]), "+v"(h[1]), "+v"(h[2]), "+v"(h[3]), "+v"(h[4]), "+v"(h[5]), "+v"(h[6]), "+v"(h[7]));
    _Pragma("unroll 1") for (int it = 0; it < SPIN_CAP; ++it) {
      unsigned m = 0;
#pragma unroll
      for (int p = 0; p < 8; ++p) m = top(m, h[p]);
      if (!absent(m)) break;
      __builtin_amdgcn_s_sleep(1);
#pragma unroll
      for (int p = 0; p < 8; ++p) refill(h[p], v48, sWs, XB_OFF + (rt * 8 + p) * 46080 + 36864 + cb * 768);
      asm volatile("s_waitcnt vmcnt(0)" : "+v"(h[0]), "+v"(h[1]), "+v"(h[2]), "+v"(h[3]), "+v"(h[4]), "+v"(h[5]), "+v"(h[6]), "+v"(h[7]));
    }
    if (lane < 48) {
#pragma unroll
      for (int p = 0; p < 8; ++p) {
        *(LAS u32x2*)(L + (olo ^ ((p & 3) * 64)) + (p >> 2) * 2048) = u32x2{h[p].x, h[p].y};
        *(LAS u32x2*)(L + (ohi ^ ((p & 3) * 64)) + (p >> 2) * 2048) = u32x2{h[p].z, h[p].w};
      }
    }
  }
#pragma unroll
  for (int i = 0; i < 18; ++i) PIN(Rw[i]);
  BAR();
  if (w == 11 && vl16 == 0) __hip_atomic_store((LAG unsigned*)(ws + G3_OFF + pc * 4), (unsigned)tag, __ATOMIC_RELAXED, __HIP_MEMORY_SCOPE_AGENT);
  f32x4 am[4][3];
  u32x4 g3;
  {
    sfor<2>([&](auto PH) {
      constexpr int ph = PH;
      LAS char* A = L + ph * L_H1 + (vl16 ^ ((fresh_lane() & 16) * 12));
      u32x4 Bf[2][4];
#pragma unroll
      for (int mb = 0; mb < 4; ++mb) Bf[0][mb] = *(LAS u32x4*)(A + mb * 1024);
      sfor<18>([&](auto KB) {
        constexpr int kb = KB;
        if (kb < 17) {
#pragma unroll
          for (int mb = 0; mb < 4; ++mb) Bf[(kb + 1) & 1][mb] = *(LAS u32x4*)(A + ((kb + 1) * 4 + mb) * 1024);
        }
        SCHED();
        sfor<3>([&](auto J) {
          constexpr int j = J, it = ph * 54 + kb * 3 + j;
          if constexpr (it >= 18) asm volatile("s_waitcnt vmcnt(%1)" : "+v"(Rw[it % 18]) : "i"(younger(it)));
#pragma unroll
          for (int mb = 0; mb < 4; ++mb) mac(am[mb][j], Rw[it % 18], Bf[kb & 1][mb], kb == 0);
          SCHED();
          constexpr int j0 = issued(it), j1 = issued(it + 1);
          sfor<j1 - j0>([&](auto JJ) {
            constexpr int jj = j0 + JJ;
            refill(Rw[jj % 18], vl16, sWs, w_at(jj + 18));
          });
          if constexpr (it == X_W2) touch(wm, 1);
          if constexpr (it == 53) PIN(wm);
          if constexpr (it == X_OP) fill<1>(g3, vl16, sWs, G3_OFF);
          SCHED();
        });
      });
      MFMA_DONE();
      if constexpr (ph == 0) {
        const int fp = ((3 * w + 1) >> 1) - rot, fh = ((3 * (w & ~1) + 2) >> 1) - rot;
        LAS char* Hp = A + L_H1 + (fp < 0 ? fp + 18 : fp) * 4096;
        LAS char* Hh = A + L_H1 + (fh < 0 ? fh + 18 : fh) * 4096 + (w & 1) * 8;
#pragma unroll
        for (int mb = 0; mb < 4; ++mb) {
          *(LAS u32x4*)(Hp + mb * 1024) = pk8(relu4(am[mb][0]), relu4(am[mb][1]));
          *(LAS u32x2*)(Hh + mb * 1024) = pk4(relu4(am[mb][2]));
        }
        BAR();
      }
    });
  }
  {
    const int lane = fresh_lane(), v48 = lane < 48 ? vl16 : OOB;
    auto inbox = [&](int p, int n) { return n < 5 ? XB_OFF + p * 46080 + n * 9216 : AB_OFF + p * 18432 + (n - 5) * 9216; };
    PIN(g3);
    _Pragma("unroll 1") for (int it = 0; it < SPIN_CAP; ++it) {
      const unsigned tl = (unsigned)tag;
      if (__builtin_amdgcn_ballot_w64((((g3.x ^ tl) | (g3.y ^ tl)) | ((g3.z ^ tl) | (g3.w ^ tl))) != 0) == 0) break;
      __builtin_amdgcn_s_sleep(2);
      refill(g3, vl16, sWs, G3_OFF);
      asm volatile("s_waitcnt vmcnt(0)" : "+v"(g3));
    }
    const int vq = (lane >> 4) * 64 + (lane & 3) * 16;
    LAS char* keep = L + w * 768;
#pragma unroll
    for (int h = 0; h < 2; ++h) {
      const int p = 4 * h + ((lane >> 2) & 3), n = (sl - p - 1) & 7, vo = n < 7 ? inbox(rt * 8 + p, n) + vq : OOB;
      const u32x4 o0 = odd8(am[2 * h][0], am[2 * h][1]), o1 = odd8(am[2 * h + 1][0], am[2 * h + 1][1]), o2 = odd8(am[2 * h][2], am[2 * h + 1][2]);
      cst128(o0, rWs, vo, w * 768);
      cst128(o1, rWs, vo, w * 768 + 256);
      cst128(o2, rWs, vo, w * 768 + 512);
      if (n == 7) {
        *(LAS u32x4*)(keep + vq) = o0;
        *(LAS u32x4*)(keep + vq + 256) = o1;
        *(LAS u32x4*)(keep + vq + 512) = o2;
      }
    }
    BAR();
    int flo, fhi;
    bool alo, ahi;
    fin_map(w, lane, flo, fhi, alo, ahi);
    const u32x4 x2 = *(LAS u32x4*)(L + L_X2 + w * 768 + vl16);
    const f32x4 gl = *(LAS f32x4*)(L + L_GB + flo * 4), gh = *(LAS f32x4*)(L + L_GB + fhi * 4);
    u32x4 p[8], plo, phi;
    p[7] = *(LAS u32x4*)(keep + (lane < 48 ? vl16 : 0));
    pickers(plo, phi);
    _Pragma("unroll 1") for (int it = 0; it < SPIN_CAP; ++it) {
#pragma unroll
      for (int k = 0; k < 7; ++k) fill<1>(p[k], v48, sWs, inbox(pc, k) + w * 768);
      asm volatile("s_waitcnt vmcnt(0)" : "+v"(p[0]), "+v"(p[1]), "+v"(p[2]), "+v"(p[3]), "+v"(p[4]), "+v"(p[5]), "+v"(p[6]));
      u32x4 m = p[0];
#pragma unroll
      for (int k = 1; k < 7; ++k) m &= p[k];
      if (__builtin_amdgcn_ballot_w64(lane < 48 && (m.x & m.y & m.z & m.w & 1u) == 0) == 0) break;
      __builtin_amdgcn_s_sleep(2);
    }
    f32x4 sl4, sh4, sl5, sh5;
    SCHED();
#pragma unroll
    for (int k = 0; k < 8; k += 2) {
      mac(sl4, plo, p[k], k == 0);
      mac(sh4, phi, p[k], k == 0);
      mac(sl5, plo, p[k + 1], k == 0);
      mac(sh5, phi, p[k + 1], k == 0);
    }
    MFMA_DONE();
    sl4 += sl5;
    sh4 += sh5;
    const rsrc_t rO = mkrs(out, S * D * 2);
    const int olo = lane < 48 ? (16 * alo + (lane & 3)) * 1152 + flo * 2 : OOB, ohi = lane < 48 ? (16 * ahi + (lane & 3)) * 1152 + fhi * 2 : OOB;
    __builtin_amdgcn_raw_buffer_store_b64(pk4(up4(x2.x, x2.y) + gl * sl4), rO, olo, row0 * 1152, 0);
    __builtin_amdgcn_raw_buffer_store_b64(pk4(up4(x2.z, x2.w) + gh * sh4), rO, ohi, row0 * 1152, 0);
  }
  asm volatile(".Lfb_hi:");
}

extern "C" void kernel_launch(void* const* d_in, const int* in_sizes, int n_in, void* d_out, int out_size, void* d_ws, size_t ws_size,
                              hipStream_t stream) {
  (void)hipFuncSetAttribute((const void*)fused_block, hipFuncAttributeMaxDynamicSharedMemorySize, LDS_BYTES);
  fused_block<<<256, 768, LDS_BYTES, stream>>>((const u16*)d_in[0], (const u16*)d_in[1], (const u16*)d_in[2], (const u16*)d_in[3],
                                               (const u16*)d_in[4], (const u16*)d_in[6], (const u16*)d_in[7], (const u16*)d_in[8],
                                               (const u16*)d_in[9], (char*)d_ws, (u16*)d_out);
}
```
